```python
import torch

def _softmax(x, axis=-1):
    return torch.softmax(x, dim=axis)
def _gelu(x):
    return torch.nn.functional.gelu(x, approximate="tanh")
def _silu(x):
    return torch.nn.functional.silu(x)
def _sigmoid(x):
    return torch.sigmoid(x)
def _layernorm(x, axis=-1, eps=1e-5):
    m = x.mean(dim=axis, keepdim=True)
    v = x.var(dim=axis, keepdim=True, unbiased=False)
    return (x - m) / torch.sqrt(v + eps)
def _scatter_add(tgt, idx, src, axis=0):
    return tgt.clone().index_add_(axis, idx, src)
def _segment_sum(src, seg, n):
    out = torch.zeros((n,) + src.shape[1:], dtype=src.dtype)
    return out.index_add_(0, seg, src)
def _pad(x, pw):
    flat = tuple(v for pair in reversed(pw) for v in pair)
    return torch.nn.functional.pad(x, flat)

kernel_name = 'recurrent_scan__linear_attn_block__glu_gated_swiglu__prompt'

def setup_inputs():

    _w = (lambda a: (lambda t: t.to(torch.bfloat16) if t.is_floating_point() else t)(torch.as_tensor(a)))
    def _A_setup():
        B, S, D = 1, 2048, 448
        return {
            "x": _w((lambda *s: torch.randn(*s, dtype=torch.bfloat16) * 0.05)(B, S, D)),
            "wqkv": _w((lambda *s: torch.randn(*s, dtype=torch.bfloat16) * 0.8)(D, 3 * D)),
            "wo": _w((lambda *s: torch.randn(*s, dtype=torch.bfloat16) * 0.8)(D, D)),
        }
    _A_in = _A_setup()

    def _B_setup():
        B, S, D, F = 1, 2048, 448, 1792
        return {
            "x": _w((lambda *s: torch.randn(*s, dtype=torch.bfloat16) * 0.05)(B, S, D)),
            "w1": _w((lambda *s: torch.randn(*s, dtype=torch.bfloat16) * 0.2)(D, F) * (2.0 / D) ** 0.5),
            "w3": _w((lambda *s: torch.randn(*s, dtype=torch.bfloat16) * 0.2)(D, F) * (2.0 / D) ** 0.5),
            "w2": _w((lambda *s: torch.randn(*s, dtype=torch.bfloat16) * 0.2)(F, D) * (2.0 / F) ** 0.5),
        }
    _B_in = _B_setup()

    return {
        "A_x": _A_in["x"],
        "A_wqkv": _A_in["wqkv"],
        "A_wo": _A_in["wo"],
        "B_x": _B_in["x"],
        "B_w1": _B_in["w1"],
        "B_w3": _B_in["w3"],
        "B_w2": _B_in["w2"],
    }

def reference(A_x, A_wqkv, A_wo, B_x, B_w1, B_w3, B_w2):
    def _A_ref(x, wqkv, wo):
        B, S, D = x.shape
        H = 16; Dh = D // H
        qkv = (lambda x, *d: x.permute(*d))((x @ wqkv).reshape(B, S, 3, H, Dh), 2, 0, 3, 1, 4)
        q, k, v = qkv[0], qkv[1], qkv[2]
        phi = lambda t: torch.where(t > 0, t + 1, torch.exp(t)) + 1e-6
        pq, pk = phi(q), phi(k)
        kv = torch.einsum("bhsd,bhse->bhde", pk, v)
        z = torch.einsum("bhsd->bhd", pk)
        num = torch.einsum("bhqd,bhde->bhqe", pq, kv)
        den = torch.einsum("bhqd,bhd->bhq", pq, z)[..., None] + 1e-6
        o = num / den
        return (lambda x, *d: x.permute(*d))(o, 0, 2, 1, 3).reshape(B, S, D) @ wo

    def _B_ref(x, w1, w3, w2):
        gate = torch.einsum("bsd,df->bsf", x, w1)
        up = torch.einsum("bsd,df->bsf", x, w3)
        h = _silu(gate) * up
        out = torch.einsum("bsf,fd->bsd", h, w2)
        return out

    x = A_x
    xn = _layernorm(x, axis=-1)
    s = _A_ref(xn, A_wqkv, A_wo)
    g = _silu(_B_ref(xn, B_w1, B_w3, B_w2))
    return x + s * g
```

```cpp
#include <hip/hip_runtime.h>
#include <hip/hip_bf16.h>
#include <stdint.h>

typedef short    bf16x8 __attribute__((ext_vector_type(8)));
typedef short    bf16x4 __attribute__((ext_vector_type(4)));
typedef float    f32x4  __attribute__((ext_vector_type(4)));
typedef float    f32x2  __attribute__((ext_vector_type(2)));
typedef unsigned u32x4  __attribute__((ext_vector_type(4)));
typedef unsigned u32x2  __attribute__((ext_vector_type(2)));
typedef __bf16   bf2_t  __attribute__((ext_vector_type(2)));
#define LAS __attribute__((address_space(3)))

constexpr int S_ = 2048, D_ = 448, F_ = 1792;
constexpr size_t WS_H   = 0;
constexpr size_t WS_PQ  = WS_H + (size_t)S_ * F_ * 2;
constexpr size_t WS_P   = WS_PQ + (size_t)S_ * 512 * 2;
constexpr size_t WS_KVT = WS_P + (size_t)16 * 16 * 1024 * 4;
constexpr size_t WS_CNT = WS_KVT + (size_t)16 * 1024 * 2;

__device__ __forceinline__ unsigned pack_bf16(float lo, float hi) {
  f32x2 v = {lo, hi};
  bf2_t r = __builtin_convertvector(v, bf2_t);
  return __builtin_bit_cast(unsigned, r);
}
__device__ __forceinline__ float bf_lo(unsigned u) { return __uint_as_float(u << 16); }
__device__ __forceinline__ float bf_hi(unsigned u) { return __uint_as_float(u & 0xffff0000u); }
__device__ __forceinline__ float rbf(float f) { return bf_lo(pack_bf16(f, 0.f) & 0xffffu); }
__device__ __forceinline__ float silu_f(float v) { return v * __builtin_amdgcn_rcpf(1.f + __expf(-v)); }
__device__ __forceinline__ float phi_f(float acc) {
  float t = rbf(acc);
  float a = t > 0.f ? t + 1.f : __expf(t);
  return rbf(rbf(a) + 1e-6f);
}
__device__ __forceinline__ f32x4 mfma16(bf16x8 a, bf16x8 b, f32x4 c) {
  return __builtin_amdgcn_mfma_f32_16x16x32_bf16(a, b, c, 0, 0, 0);
}
__device__ __forceinline__ bf16x4 tr_read(unsigned lds_addr) {
  return __builtin_amdgcn_ds_read_tr16_b64_v4i16((LAS bf16x4*)(uintptr_t)lds_addr);
}
__device__ __forceinline__ bf16x8 cat8(bf16x4 lo, bf16x4 hi) {
  return __builtin_shufflevector(lo, hi, 0, 1, 2, 3, 4, 5, 6, 7);
}
__device__ __forceinline__ void dma16(const void* gsrc, unsigned lds_dst) {
  unsigned keep;
  asm volatile("s_mov_b32 %0, m0\n\ts_mov_b32 m0, %2\n\ts_nop 0\n\tglobal_load_lds_dwordx4 %1, off\n\ts_mov_b32 m0, %0"
               : "=&s"(keep) : "v"(gsrc), "s"(lds_dst) : "memory");
}
__device__ __forceinline__ void dma16s(unsigned voff, const void* sbase, unsigned lds_dst) {
  unsigned keep;
  asm volatile("s_mov_b32 %0, m0\n\ts_mov_b32 m0, %3\n\ts_nop 0\n\tglobal_load_lds_dwordx4 %1, %2\n\ts_mov_b32 m0, %0"
               : "=&s"(keep) : "v"(voff), "s"(sbase), "s"(lds_dst) : "memory");
}
#define GLOAD16(dst, ptr, off) \
  asm volatile("global_load_dwordx4 %0, %1, off offset:%2" : "=v"(dst) : "v"(ptr), "i"(off) : "memory")
#define GLOAD16S(dst, voff, sbase, off) \
  asm volatile("global_load_dwordx4 %0, %1, %2 offset:%3" : "=v"(dst) : "v"(voff), "s"(sbase), "i"(off) : "memory")
#define WAITVM(n) asm volatile("s_waitcnt vmcnt(" #n ")" ::: "memory")
#define BARRIER() asm volatile("s_barrier" ::: "memory")

constexpr int K1_STAGE = 40960;

__global__ __launch_bounds__(512, 2) void k1_kernel(const char* __restrict__ x, const char* __restrict__ wqkv,
                                                   const char* __restrict__ w1, const char* __restrict__ w3,
                                                   char* __restrict__ ws) {
  __shared__ __attribute__((aligned(16))) char smem[4 * K1_STAGE];
  const int tid = threadIdx.x, lane = tid & 63;
  const int w = __builtin_amdgcn_readfirstlane(tid >> 6);
  const int wm = w & 3, wn = w >> 2;
  const int b = blockIdx.x, xcd = b & 7, idx = b >> 3;
  const int j = 2 * xcd + (idx & 1), mt = idx >> 1;
  const int m = lane & 15, g = lane >> 4, q = (lane >> 2) & 3, p = lane & 3;
  const unsigned lds0 = (unsigned)(uintptr_t)(LAS char*)smem;

  if (b == 0 && tid == 0) *(unsigned*)(ws + WS_CNT) = 0u;

  u32x4 a[2][14];
  {
    const char* xp0 = x + (size_t)(mt * 128 + wm * 32 + m) * 896 + g * 16;
    const char* xp1 = xp0 + 16 * 896;
#pragma unroll
    for (int s = 0; s < 14; ++s) { GLOAD16(a[0][s], xp0, 64 * s); GLOAD16(a[1][s], xp1, 64 * s); }
  }
  const char* src[5];
  unsigned inc[5];
#pragma unroll
  for (int u = 0; u < 5; ++u) {
    const int o = 1024 * (w * 5 + u) + 16 * lane;
    const int r = o / 640, cb = o - r * 640;
    const int grp = cb >> 7, pp = (cb >> 5) & 3, half = (cb >> 4) & 1;
    const int tn = grp * 4 + (pp ^ ((r >> 1) & 3));
    const int rs = r & 31;
    const int k = (r & 32) + 8 * ((rs >> 2) & 3) + 4 * (rs >> 4) + (rs & 3);
    const char* base; int col, rowb;
    if (tn < 5)       { base = w1;   col = 112 * j + 16 * tn;             rowb = 3584; }
    else if (tn < 10) { base = w3;   col = 112 * j + 16 * (tn - 5);       rowb = 3584; }
    else if (tn < 12) { base = w1;   col = 112 * j + 16 * (tn - 5);       rowb = 3584; }
    else if (tn < 14) { base = w3;   col = 112 * j + 16 * (tn - 7);       rowb = 3584; }
    else if (tn < 16) { base = wqkv; col = 28 * j + 16 * (tn - 14);       rowb = 2688; }
    else if (tn < 18) { base = wqkv; col = 448 + 28 * j + 16 * (tn - 16); rowb = 2688; }
    else              { base = wqkv; col = 892 + 28 * j + 16 * (tn - 18); rowb = 2688; }
    src[u] = base + (size_t)k * rowb + (col + 8 * half) * 2;
    inc[u] = 64 * rowb;
  }
#pragma unroll
  for (int s = 0; s < 3; ++s) {
#pragma unroll
    for (int u = 0; u < 5; ++u) {
      dma16(src[u], lds0 + s * K1_STAGE + (w * 5 + u) * 1024);
      src[u] += inc[u];
    }
  }
  asm volatile("s_waitcnt vmcnt(15)"
               : "+v"(a[0][0]), "+v"(a[0][1]), "+v"(a[0][2]), "+v"(a[0][3]), "+v"(a[0][4]), "+v"(a[0][5]), "+v"(a[0][6]),
                 "+v"(a[0][7]), "+v"(a[0][8]), "+v"(a[0][9]), "+v"(a[0][10]), "+v"(a[0][11]), "+v"(a[0][12]), "+v"(a[0][13])
               :: "memory");
  asm volatile(""
               : "+v"(a[1][0]), "+v"(a[1][1]), "+v"(a[1][2]), "+v"(a[1][3]), "+v"(a[1][4]), "+v"(a[1][5]), "+v"(a[1][6]),
                 "+v"(a[1][7]), "+v"(a[1][8]), "+v"(a[1][9]), "+v"(a[1][10]), "+v"(a[1][11]), "+v"(a[1][12]), "+v"(a[1][13])
               :: "memory");
  __builtin_amdgcn_sched_barrier(0);

  float rr[2], cc[2];
#pragma unroll
  for (int i = 0; i < 2; ++i) {
    float s1 = 0.f, s2 = 0.f;
    const bf2_t ones = __builtin_bit_cast(bf2_t, 0x3f803f80u);
#pragma unroll
    for (int s = 0; s < 14; ++s)
#pragma unroll
      for (int e = 0; e < 4; ++e) {
        const unsigned uu = a[i][s][e];
        bf2_t v = __builtin_bit_cast(bf2_t, uu);
        s1 = __builtin_amdgcn_fdot2_f32_bf16(v, ones, s1, false);
        s2 = __builtin_amdgcn_fdot2_f32_bf16(v, v, s2, false);
      }
    s1 += __shfl_xor(s1, 16); s1 += __shfl_xor(s1, 32);
    s2 += __shfl_xor(s2, 16); s2 += __shfl_xor(s2, 32);
    const float mean = s1 * (1.f / 448.f);
    const float var = fmaxf(s2 * (1.f / 448.f) - mean * mean, 0.f);
    const float sd = rbf(sqrtf(rbf(rbf(var) + 1e-5f)));
    rr[i] = 1.f / sd;
    cc[i] = -rbf(mean) * rr[i];
  }

  const int sig = (2 * g + (q >> 1)) & 3;
  unsigned lb[4];
#pragma unroll
  for (int xx = 0; xx < 4; ++xx)
    lb[xx] = lds0 + (4 * g + q) * 640 + ((((xx + 2 * wn) & 3) ^ sig) * 32) + 8 * p + (wn ? (xx < 2 ? 256 : 384) : 0);

  f32x4 acc[10][2];
#pragma unroll
  for (int t = 0; t < 10; ++t) { acc[t][0] = f32x4{0.f, 0.f, 0.f, 0.f}; acc[t][1] = f32x4{0.f, 0.f, 0.f, 0.f}; }

#pragma unroll
  for (int s = 0; s < 7; ++s) {
    if (s < 5) WAITVM(10); else if (s == 5) WAITVM(5); else WAITVM(0);
    BARRIER();
    if (s + 3 < 7) {
#pragma unroll
      for (int u = 0; u < 5; ++u) {
        dma16(src[u], lds0 + ((s + 3) & 3) * K1_STAGE + (w * 5 + u) * 1024);
        src[u] += inc[u];
        asm volatile("" : "+v"(src[u]));
      }
    }
#pragma unroll
    for (int ks = 0; ks < 2; ++ks) {
      const int kk = 2 * s + ks;
      bf16x8 xa[2];
      asm volatile("" : "+v"(a[0][kk]), "+v"(a[1][kk]));
#pragma unroll
      for (int i = 0; i < 2; ++i) {
        u32x4 o;
#pragma unroll
        for (int e = 0; e < 4; ++e) {
          unsigned u = a[i][kk][e];
          o[e] = pack_bf16(fmaf(bf_lo(u), rr[i], cc[i]), fmaf(bf_hi(u), rr[i], cc[i]));
        }
        xa[i] = __builtin_bit_cast(bf16x8, o);
      }
#pragma unroll
      for (int ti = 0; ti < 10; ++ti) {
        const unsigned ad = lb[ti & 3] + (s & 3) * K1_STAGE + (32 * ks) * 640 + (ti >> 2) * 128;
        bf16x8 wf = cat8(tr_read(ad), tr_read(ad + 16 * 640));
        acc[ti][0] = mfma16(wf, xa[0], acc[ti][0]);
        acc[ti][1] = mfma16(wf, xa[1], acc[ti][1]);
      }
      __builtin_amdgcn_sched_barrier(0);
    }
  }

  const int row0 = mt * 128 + wm * 32 + m;
  auto store_h = [&](const f32x4& ga4, const f32x4& up4, int i, int pidx) {
    float hv[4];
#pragma unroll
    for (int r = 0; r < 4; ++r) hv[r] = rbf(silu_f(rbf(ga4[r]))) * rbf(up4[r]);
    u32x2 o = {pack_bf16(hv[0], hv[1]), pack_bf16(hv[2], hv[3])};
    *(u32x2*)(ws + WS_H + ((size_t)(row0 + 16 * i) * F_ + 112 * j + 16 * pidx + 4 * g) * 2) = o;
  };
  BARRIER();
  if (wn == 0) {
#pragma unroll
    for (int pp = 0; pp < 5; ++pp)
#pragma unroll
      for (int i = 0; i < 2; ++i) store_h(acc[pp][i], acc[5 + pp][i], i, pp);
  } else {
#pragma unroll
    for (int c = 0; c < 2; ++c)
#pragma unroll
      for (int i = 0; i < 2; ++i) {
        float kf[4], vf[4];
#pragma unroll
        for (int r = 0; r < 4; ++r) {
          kf[r] = (c == 1 && g == 3) ? 0.f : phi_f(acc[6 + c][i][r]);
          vf[r] = (c == 0 && g == 0) ? (r == 0 ? 1.f : 0.f) : acc[8 + c][i][r];
        }
        const int r = wm * 32 + 16 * i + m, sw = (r >> 1) & 3;
        *(u32x2*)(smem + r * 128 + ((c ^ sw) * 32) + 8 * g) = u32x2{pack_bf16(kf[0], kf[1]), pack_bf16(kf[2], kf[3])};
        *(u32x2*)(smem + r * 128 + (((2 + c) ^ sw) * 32) + 8 * g) = u32x2{pack_bf16(vf[0], vf[1]), pack_bf16(vf[2], vf[3])};
      }
#pragma unroll
    for (int pp = 0; pp < 2; ++pp)
#pragma unroll
      for (int i = 0; i < 2; ++i) store_h(acc[pp][i], acc[2 + pp][i], i, 5 + pp);
#pragma unroll
    for (int c = 0; c < 2; ++c)
#pragma unroll
      for (int i = 0; i < 2; ++i) {
        float pv[4];
#pragma unroll
        for (int r = 0; r < 4; ++r) pv[r] = (c == 1 && g == 3) ? 0.f : phi_f(acc[4 + c][i][r]);
        u32x2 o = {pack_bf16(pv[0], pv[1]), pack_bf16(pv[2], pv[3])};
        *(u32x2*)(ws + WS_PQ + ((size_t)(row0 + 16 * i) * 512 + 32 * j + 16 * c + 4 * g) * 2) = o;
      }
  }
  __syncthreads();
  if (wn == 0) {
    const int dc = wm >> 1, ec = wm & 1;
    const unsigned la = lds0 + (4 * g + q) * 128 + ((dc ^ sig) * 32) + 8 * p;
    const unsigned lv = lds0 + (4 * g + q) * 128 + (((2 + ec) ^ sig) * 32) + 8 * p;
    f32x4 kv = f32x4{0.f, 0.f, 0.f, 0.f};
#pragma unroll
    for (int ks = 0; ks < 4; ++ks) {
      bf16x8 af = cat8(tr_read(la + ks * 4096), tr_read(la + ks * 4096 + 2048));
      bf16x8 bfr = cat8(tr_read(lv + ks * 4096), tr_read(lv + ks * 4096 + 2048));
      kv = mfma16(af, bfr, kv);
    }
    *(f32x4*)(ws + WS_P + ((size_t)(j * 16 + mt) * 1024 + (16 * ec + m) * 32 + 16 * dc + 4 * g) * 4) = kv;
  }
}

constexpr int K2_SLOT = 8192;
constexpr int K2_RSTR = 272;
constexpr int K2_PART = 64 * K2_RSTR;

__global__ __launch_bounds__(256, 1) void k2_kernel(const char* __restrict__ x, const char* __restrict__ wo,
                                                   const char* __restrict__ w2, char* ws, char* __restrict__ out) {
  __shared__ __attribute__((aligned(16))) char smem[8 * K2_PART];
  const int tid = threadIdx.x, lane = tid & 63;
  const int w = __builtin_amdgcn_readfirstlane(tid >> 6);
  const int b = blockIdx.x, xcd = b & 7, idx = b >> 3;
  const int mtile = xcd * 4 + (idx & 3), nt = idx >> 2;
  const int R0 = mtile * 64, C0 = nt * 64;
  const int m = lane & 15, g = lane >> 4, q = (lane >> 2) & 3, p = lane & 3;
  const unsigned lds0 = (unsigned)(uintptr_t)(LAS char*)smem;
  const unsigned ring = lds0 + w * 4 * K2_SLOT;

  unsigned aoff[4], qoff[4];
#pragma unroll
  for (int i = 0; i < 4; ++i) {
    aoff[i] = (unsigned)(WS_H + ((size_t)(R0 + 16 * i + m) * F_ + 448 * w + 8 * g) * 2);
    qoff[i] = (unsigned)(WS_PQ + ((size_t)(R0 + 16 * i + m) * 512 + 128 * w + 8 * g) * 2);
  }
  const int lr = lane >> 3, cbk = lane & 7;
  const int tnd = (cbk >> 1) ^ ((lane >> 4) & 3);
  const unsigned bcol = (unsigned)(C0 + 16 * tnd + 8 * (cbk & 1)) * 2;
  const unsigned boff = (unsigned)(8 * (lr >> 2) + (lr & 3)) * 896 + bcol;
  const int sig = (2 * g + (q >> 1)) & 3;
  unsigned lb[4];
#pragma unroll
  for (int xx = 0; xx < 4; ++xx) lb[xx] = ring + (4 * g + q) * 128 + ((xx ^ sig) * 32) + 8 * p;

  u32x4 a[4][4][2];
  auto issue = [&](int c) {
    const int sl = c & 3;
    if (c < 7) {
#pragma unroll
      for (int i = 0; i < 4; ++i) {
        GLOAD16S(a[sl][i][0], aoff[i], ws, 128 * c);
        GLOAD16S(a[sl][i][1], aoff[i], ws, 128 * c + 64);
      }
      const char* wb = w2 + (size_t)(448 * w + 64 * c) * 896;
#pragma unroll
      for (int u = 0; u < 8; ++u) {
        const int kb = 32 * (u >> 2) + ((u & 1) ? 16 : 0) + ((u & 2) ? 4 : 0);
        dma16s(boff, wb + kb * 896, ring + sl * K2_SLOT + u * 1024);
      }
    } else {
#pragma unroll
      for (int i = 0; i < 4; ++i) {
        GLOAD16S(a[sl][i][0], qoff[i], ws, 128 * (c - 7));
        GLOAD16S(a[sl][i][1], qoff[i], ws, 128 * (c - 7) + 64);
      }
#pragma unroll
      for (int u = 0; u < 8; ++u) {
        const int r = 8 * u + lr;
        const int e = min(r & 31, 27);
        const unsigned vo = (unsigned)(28 * (4 * w + 2 * (c - 7) + (r >> 5)) + e) * 896 + bcol;
        dma16s(vo, wo, ring + sl * K2_SLOT + u * 1024);
      }
    }
  };

  issue(0); issue(1); issue(2);

  if (b < 64 && w == 3) {
    const int head = b >> 2, pos = 8 * (b & 3) + (lane >> 3), d4 = (lane & 7) * 4;
    const float* P = (const float*)(ws + WS_P) + (size_t)head * 16 * 1024 + pos * 32 + d4;
    f32x4 v[16];
#pragma unroll
    for (int t = 0; t < 16; ++t) v[t] = *(const f32x4*)(P + t * 1024);
    f32x4 sacc = v[0];
#pragma unroll
    for (int t = 1; t < 16; ++t) sacc += v[t];
    const int e = pos >= 4 ? pos - 4 : pos + 28;
    unsigned long long o = (unsigned long long)pack_bf16(sacc[0], sacc[1]) |
                           ((unsigned long long)pack_bf16(sacc[2], sacc[3]) << 32);
    __hip_atomic_store((unsigned long long*)(ws + WS_KVT + ((size_t)(head * 32 + e) * 32 + d4) * 2), o,
                       __ATOMIC_RELAXED, __HIP_MEMORY_SCOPE_AGENT);
    WAITVM(0);
    if (lane == 0) __hip_atomic_fetch_add((unsigned*)(ws + WS_CNT), 1u, __ATOMIC_RELAXED, __HIP_MEMORY_SCOPE_AGENT);
  }

  f32x4 acc[4][4], acs[4][4];
#pragma unroll
  for (int i = 0; i < 4; ++i)
#pragma unroll
    for (int t = 0; t < 4; ++t) { acc[i][t] = f32x4{0.f, 0.f, 0.f, 0.f}; acs[i][t] = f32x4{0.f, 0.f, 0.f, 0.f}; }

#define K2_PIN(sl)                                                                                              \
  asm volatile("" : "+v"(a[sl][0][0]), "+v"(a[sl][0][1]), "+v"(a[sl][1][0]), "+v"(a[sl][1][1]), "+v"(a[sl][2][0]), \
               "+v"(a[sl][2][1]), "+v"(a[sl][3][0]), "+v"(a[sl][3][1]) :: "memory");                            \
  __builtin_amdgcn_sched_barrier(0)

#pragma unroll
  for (int c = 0; c < 7; ++c) {
    const int sl = c & 3;
    if (c < 6) WAITVM(32); else WAITVM(32);
    K2_PIN(sl);
    if (c + 3 < 9) issue(c + 3);
#pragma unroll
    for (int ks = 0; ks < 2; ++ks) {
#pragma unroll
      for (int t = 0; t < 4; ++t) {
        const unsigned ad = lb[t] + sl * K2_SLOT + 32 * ks * 128;
        bf16x8 wf = cat8(tr_read(ad), tr_read(ad + 16 * 128));
#pragma unroll
        for (int i = 0; i < 4; ++i) acc[i][t] = mfma16(wf, __builtin_bit_cast(bf16x8, a[sl][i][ks]), acc[i][t]);
      }
    }
    if (c == 5) {   }
  }

  {
    const unsigned* cnt = (const unsigned*)(ws + WS_CNT);
    while (__hip_atomic_load(cnt, __ATOMIC_RELAXED, __HIP_MEMORY_SCOPE_AGENT) < 64u) __builtin_amdgcn_s_sleep(2);
  }
  u32x4 kvf[4][2];
  {
    const unsigned ko = (unsigned)(WS_KVT + ((size_t)(4 * w * 32 + m) * 32 + 8 * g) * 2);
#pragma unroll
    for (int hh = 0; hh < 4; ++hh)
#pragma unroll
      for (int ec = 0; ec < 2; ++ec)
        asm volatile("global_load_dwordx4 %0, %1, %2 offset:%3 sc1" : "=v"(kvf[hh][ec]) : "v"(ko + (hh >> 1) * 4096), "s"(ws),
                     "i"(((hh & 1) * 32 + ec * 16) * 64) : "memory");
    asm volatile("s_waitcnt vmcnt(0)"
                 : "+v"(kvf[0][0]), "+v"(kvf[0][1]), "+v"(kvf[1][0]), "+v"(kvf[1][1]), "+v"(kvf[2][0]), "+v"(kvf[2][1]),
                   "+v"(kvf[3][0]), "+v"(kvf[3][1]) :: "memory");
  }
  K2_PIN(3);
  K2_PIN(0);
#pragma unroll
  for (int hh = 0; hh < 4; ++hh) {
    const int sl = (7 + (hh >> 1)) & 3, ks = hh & 1;
    bf16x8 wf[4];
#pragma unroll
    for (int t = 0; t < 4; ++t) {
      const unsigned ad = lb[t] + sl * K2_SLOT + 32 * ks * 128;
      wf[t] = cat8(tr_read(ad), tr_read(ad + 16 * 128));
    }
#pragma unroll
    for (int i = 0; i < 4; ++i) {
      const bf16x8 pq = __builtin_bit_cast(bf16x8, a[sl][i][ks]);
      f32x4 n0 = mfma16(__builtin_bit_cast(bf16x8, kvf[hh][0]), pq, f32x4{0.f, 0.f, 0.f, 0.f});
      f32x4 n1 = mfma16(__builtin_bit_cast(bf16x8, kvf[hh][1]), pq, f32x4{0.f, 0.f, 0.f, 0.f});
      const float den = rbf(rbf(__shfl(n1[0], 48 + m)) + 1e-6f);
      float o0[4], o1[4];
#pragma unroll
      for (int r = 0; r < 4; ++r) {
        o0[r] = rbf(n0[r]) / den;
        o1[r] = g == 3 ? 0.f : rbf(n1[r]) / den;
      }
      u32x4 ob = {pack_bf16(o0[0], o0[1]), pack_bf16(o0[2], o0[3]), pack_bf16(o1[0], o1[1]), pack_bf16(o1[2], o1[3])};
      const bf16x8 of = __builtin_bit_cast(bf16x8, ob);
#pragma unroll
      for (int t = 0; t < 4; ++t) acs[i][t] = mfma16(wf[t], of, acs[i][t]);
    }
  }

  __syncthreads();
#pragma unroll
  for (int i = 0; i < 4; ++i)
#pragma unroll
    for (int t = 0; t < 4; ++t) {
      char* pb = smem + (16 * i + m) * K2_RSTR + (16 * t + 4 * g) * 4;
      *(f32x4*)(pb + (2 * w) * K2_PART) = acc[i][t];
      *(f32x4*)(pb + (2 * w + 1) * K2_PART) = acs[i][t];
    }
  __syncthreads();
  {
    const int row = tid >> 2, n0 = (tid & 3) * 16;
    const size_t go = ((size_t)(R0 + row) * D_ + C0 + n0) * 2;
    const u32x4 xv0 = *(const u32x4*)(x + go), xv1 = *(const u32x4*)(x + go + 16);
    unsigned res[8];
#pragma unroll
    for (int v4 = 0; v4 < 4; ++v4) {
      const char* pb = smem + row * K2_RSTR + (n0 + 4 * v4) * 4;
      f32x4 so = *(const f32x4*)(pb), ss = *(const f32x4*)(pb + K2_PART);
#pragma unroll
      for (int u = 1; u < 4; ++u) {
        so += *(const f32x4*)(pb + (2 * u) * K2_PART);
        ss += *(const f32x4*)(pb + (2 * u + 1) * K2_PART);
      }
      float y[4];
#pragma unroll
      for (int r = 0; r < 4; ++r) {
        const unsigned xu = v4 < 2 ? xv0[2 * v4 + (r >> 1)] : xv1[2 * (v4 - 2) + (r >> 1)];
        const float xf = (r & 1) ? bf_hi(xu) : bf_lo(xu);
        const float gg = rbf(silu_f(rbf(so[r])));
        y[r] = xf + rbf(rbf(ss[r]) * gg);
      }
      res[2 * v4] = pack_bf16(y[0], y[1]);
      res[2 * v4 + 1] = pack_bf16(y[2], y[3]);
    }
    *(u32x4*)(out + go) = u32x4{res[0], res[1], res[2], res[3]};
    *(u32x4*)(out + go + 16) = u32x4{res[4], res[5], res[6], res[7]};
  }
}

extern "C" void kernel_launch(void* const* d_in, const int* in_sizes, int n_in,
                              void* d_out, int out_size, void* d_ws, size_t ws_size,
                              hipStream_t stream) {
  const char* x    = (const char*)d_in[0];
  const char* wqkv = (const char*)d_in[1];
  const char* wo   = (const char*)d_in[2];
  const char* w1   = (const char*)d_in[4];
  const char* w3   = (const char*)d_in[5];
  const char* w2   = (const char*)d_in[6];
  k1_kernel<<<256, 512, 0, stream>>>(x, wqkv, w1, w3, (char*)d_ws);
  k2_kernel<<<224, 256, 0, stream>>>(x, wo, w2, (char*)d_ws, (char*)d_out);
}
```

```cpp
#include <hip/hip_runtime.h>
#include <hip/hip_bf16.h>
#include <stdint.h>

typedef short    bf16x8 __attribute__((ext_vector_type(8)));
typedef short    bf16x4 __attribute__((ext_vector_type(4)));
typedef float    f32x4  __attribute__((ext_vector_type(4)));
typedef float    f32x2  __attribute__((ext_vector_type(2)));
typedef float    f32x16 __attribute__((ext_vector_type(16)));
typedef unsigned u32x4  __attribute__((ext_vector_type(4)));
typedef unsigned u32x2  __attribute__((ext_vector_type(2)));
typedef __bf16   bf2_t  __attribute__((ext_vector_type(2)));
#define LAS __attribute__((address_space(3)))

constexpr int S_ = 2048, D_ = 448, F_ = 1792;
constexpr size_t WS_H   = 0;
constexpr size_t WS_PQ  = WS_H + (size_t)S_ * F_ * 2;
constexpr size_t WS_P   = WS_PQ + (size_t)S_ * 512 * 2;
constexpr size_t WS_KVT = WS_P + (size_t)16 * 16 * 1024 * 4;
constexpr size_t WS_CNT = WS_KVT + (size_t)16 * 1024 * 2;

__device__ __forceinline__ unsigned pack_bf16(float lo, float hi) {
  f32x2 v = {lo, hi};
  bf2_t r = __builtin_convertvector(v, bf2_t);
  return __builtin_bit_cast(unsigned, r);
}
__device__ __forceinline__ float bf_lo(unsigned u) { return __uint_as_float(u << 16); }
__device__ __forceinline__ float bf_hi(unsigned u) { return __uint_as_float(u & 0xffff0000u); }
__device__ __forceinline__ float rbf(float f) { return bf_lo(pack_bf16(f, 0.f) & 0xffffu); }
__device__ __forceinline__ float silu_f(float v) { return v * __builtin_amdgcn_rcpf(1.f + __expf(-v)); }
__device__ __forceinline__ float phi_f(float t) { return (t > 0.f ? t + 1.f : __expf(t)) + 1e-6f; }
__device__ __forceinline__ f32x4 mfma16(bf16x8 a, bf16x8 b, f32x4 c) {
  return __builtin_amdgcn_mfma_f32_16x16x32_bf16(a, b, c, 0, 0, 0);
}
__device__ __forceinline__ bf16x4 tr_read(unsigned lds_addr) {
  return __builtin_amdgcn_ds_read_tr16_b64_v4i16((LAS bf16x4*)(uintptr_t)lds_addr);
}
__device__ __forceinline__ bf16x8 cat8(bf16x4 lo, bf16x4 hi) {
  return __builtin_shufflevector(lo, hi, 0, 1, 2, 3, 4, 5, 6, 7);
}
__device__ __forceinline__ void dma16(const void* gsrc, unsigned lds_dst) {
  unsigned keep;
  asm volatile("s_mov_b32 %0, m0\n\ts_mov_b32 m0, %2\n\ts_nop 0\n\tglobal_load_lds_dwordx4 %1, off\n\ts_mov_b32 m0, %0"
               : "=&s"(keep) : "v"(gsrc), "s"(lds_dst) : "memory");
}
__device__ __forceinline__ void dma16s(unsigned voff, const void* sbase, unsigned lds_dst) {
  unsigned keep;
  asm volatile("s_mov_b32 %0, m0\n\ts_mov_b32 m0, %3\n\ts_nop 0\n\tglobal_load_lds_dwordx4 %1, %2\n\ts_mov_b32 m0, %0"
               : "=&s"(keep) : "v"(voff), "s"(sbase), "s"(lds_dst) : "memory");
}
#define GLOAD16(dst, ptr, off) \
  asm volatile("global_load_dwordx4 %0, %1, off offset:%2" : "=v"(dst) : "v"(ptr), "i"(off) : "memory")
#define GLOAD16S(dst, voff, sbase, off) \
  asm volatile("global_load_dwordx4 %0, %1, %2 offset:%3" : "=v"(dst) : "v"(voff), "s"(sbase), "i"(off) : "memory")
#define WAITVM(n) asm volatile("s_waitcnt vmcnt(" #n ")" ::: "memory")
#define BARRIER() asm volatile("s_barrier" ::: "memory")
#define STAMP(i)
#define RSTAMP(i)

constexpr int K1_SLOT = 53248;
constexpr int K1_BOFF = 32768;
constexpr int K1_HP = 176;
constexpr int K1_PQO = 49152;
constexpr int K1_KVO = 73728;
constexpr int K1_CSO = 110592;
constexpr int K1_STO = 114688;
constexpr int K1_CFO = 118784;

__global__ __launch_bounds__(1024, 4) void k1_kernel(const char* __restrict__ x, const char* __restrict__ wqkv,
                                                    const char* __restrict__ w1, const char* __restrict__ w3,
                                                    char* __restrict__ ws) {
  __shared__ __attribute__((aligned(16))) char smem[3 * K1_SLOT];
  const int tid = threadIdx.x, lane = tid & 63;
  const int w = __builtin_amdgcn_readfirstlane(tid >> 6);
  const int b = blockIdx.x, xcd = b & 7, idx = b >> 3;
  const int mt = 4 * (xcd & 1) + (idx & 3), gidx = 8 * (xcd >> 1) + (idx >> 2);
  const int jj = gidx >> 1, typeB = gidx & 1;
  const int fc0 = 112 * jj + (typeB ? 80 : 0);
  const int m = lane & 15, g = lane >> 4, q = (lane >> 2) & 3, p = lane & 3;
  const int r32 = lane & 31, hh = lane >> 5;
  const int hw = w & 3;
  const unsigned lds0 = (unsigned)(uintptr_t)(LAS char*)smem;

  if (b == 0 && tid == 0) *(unsigned*)(ws + WS_CNT) = 0u;
#define DIAGOFF 0
  STAMP(0); RSTAMP(20);

  if (w >= 12) {
    __builtin_amdgcn_s_setprio(3);
    const unsigned lrow = lds0 + (64 * hw + lane) * 128;
    float s1 = 0.f, s2 = 0.f, s1b = 0.f, s2b = 0.f;
#pragma unroll
    for (int st = 0; st < 7; ++st) {
      BARRIER();
      unsigned lr2 = lrow + (st % 3) * K1_SLOT;
      asm volatile("" : "+v"(lr2));
#pragma unroll
      for (int c = 0; c < 8; ++c) {
        const u32x4 xu = *(LAS u32x4*)(uintptr_t)(lr2 + ((c ^ (lane >> 1)) & 7) * 16);
#pragma unroll
        for (int e = 0; e < 4; ++e) {
          const float lo = bf_lo(xu[e]), hi = bf_hi(xu[e]);
          s1 += lo; s1b += hi;
          s2 = fmaf(lo, lo, s2); s2b = fmaf(hi, hi, s2b);
        }
      }
      asm volatile("" : "+v"(s1), "+v"(s2), "+v"(s1b), "+v"(s2b));
    }
    s1 += s1b; s2 += s2b;
    const float mean = s1 * (1.f / 448.f);
    const float var = fmaxf(s2 * (1.f / 448.f) - mean * mean, 0.f);
    *(f32x2*)(smem + K1_STO + (64 * hw + lane) * 8) =
        f32x2{-rbf(mean), __builtin_amdgcn_rcpf(rbf(__builtin_amdgcn_sqrtf(rbf(rbf(var) + 1e-5f))))};
    __syncthreads();
    if (tid - 768 < 160) {
      const f32x4 c4 = *(const f32x4*)(smem + K1_CSO + (tid - 768) * 16);
      *(float*)(smem + K1_CFO + (tid - 768) * 4) = (c4[0] + c4[1]) + (c4[2] + c4[3]);
    }
    __syncthreads();
  } else if (w >= 8) {
    const char* xb = x + (size_t)mt * 256 * 896;
    unsigned aoff[2];
#pragma unroll
    for (int par = 0; par < 2; ++par)
      aoff[par] = (unsigned)(64 * hw + (lane >> 3)) * 896 + (((lane & 7) ^ ((4 * par + (lane >> 4)) & 7)) * 16);
#pragma unroll
    for (int u = 0; u < 8; ++u) dma16s(aoff[u & 1], xb + 8 * 896 * u, lds0 + (8 * hw + u) * 1024);
    const char* src[5];
    unsigned inc[5];
#pragma unroll
    for (int u = 0; u < 5; ++u) {
      const int o = 1024 * (5 * hw + u) + 16 * lane;
      const int r = o / 320, cb = o - r * 320;
      const int P = cb >> 5;
      const int Pm = P >= 5 ? P - 5 : P, hi = P >= 5 ? 1 : 0;
      const bool isq = typeB && Pm >= 2;
      const char* base = isq ? wqkv : (hi ? w3 : w1);
      const int col = isq ? (Pm == 2 ? 0 : (Pm == 3 ? 448 : 892)) + 28 * jj + 16 * hi : fc0 + 16 * Pm;
      src[u] = base + (size_t)r * (isq ? 2688 : 3584) + col * 2 + (cb & 16);
      inc[u] = isq ? 172032u : 229376u;
    }
    auto issueB = [&](int st) {
#pragma unroll
      for (int u = 0; u < 5; ++u) {
        dma16(src[u], lds0 + (st % 3) * K1_SLOT + K1_BOFF + (5 * hw + u) * 1024);
        src[u] += inc[u];
      }
    };
    auto issueA = [&](int st) {
#pragma unroll
      for (int u = 0; u < 8; ++u)
        dma16s(aoff[u & 1], xb + 8 * 896 * u + 128 * st, lds0 + (st % 3) * K1_SLOT + (8 * hw + u) * 1024);
    };
    issueB(0); issueA(1); issueB(1);
    const unsigned lbw = lds0 + K1_BOFF + (16 * hw + 8 * (g >> 1) + q) * 320 + (g & 1) * 160 + 8 * p;
    float cs[5] = {0.f, 0.f, 0.f, 0.f, 0.f};
#pragma unroll
    for (int st = 0; st < 7; ++st) {
      if (st < 6) WAITVM(13); else WAITVM(0);
      BARRIER();
      if (st + 2 < 7) { issueA(st + 2); issueB(st + 2); }
      unsigned lb2 = lbw + (st % 3) * K1_SLOT;
      asm volatile("" : "+v"(lb2));
#pragma unroll
      for (int ti = 0; ti < 5; ++ti) {
        const u32x4 wu = __builtin_bit_cast(u32x4, cat8(tr_read(lb2 + 32 * ti), tr_read(lb2 + 32 * ti + 4 * 320)));
#pragma unroll
        for (int e = 0; e < 4; ++e) {
          cs[ti] += bf_lo(wu[e]);
          cs[ti] += bf_hi(wu[e]);
        }
      }
      asm volatile("" : "+v"(cs[0]), "+v"(cs[1]), "+v"(cs[2]), "+v"(cs[3]), "+v"(cs[4]));
    }
#pragma unroll
    for (int ti = 0; ti < 5; ++ti) {
      const float c2 = cs[ti] + __shfl_xor(cs[ti], 32);
      if (hh == 0) *(float*)(smem + K1_CSO + (ti * 32 + r32) * 16 + hw * 4) = c2;
    }
    __syncthreads();
    __syncthreads();
  } else {
    const unsigned lb = lds0 + K1_BOFF + (8 * (g >> 1) + q) * 320 + (g & 1) * 160 + 8 * p;
    const unsigned la = lds0 + (w * 32 + r32) * 128 + ((hh ^ ((r32 >> 1) & 7)) * 16);
    f32x16 acc[5];
#pragma unroll
    for (int t = 0; t < 5; ++t)
#pragma unroll
      for (int e = 0; e < 16; ++e) acc[t][e] = 0.f;
#pragma unroll
    for (int st = 0; st < 7; ++st) {
      STAMP(2 + 2 * st);
      BARRIER();
      STAMP(3 + 2 * st);
      constexpr int PF = 4;
      unsigned lbs = lb + (st % 3) * K1_SLOT, las = la + (st % 3) * K1_SLOT;
      asm volatile("" : "+v"(lbs), "+v"(las));
      bf16x8 wf[PF + 1], xa[2];
      xa[0] = *(LAS bf16x8*)(uintptr_t)(las);
#pragma unroll
      for (int n = 0; n < PF; ++n) {
        const unsigned ad = lbs + (16 * (n / 5)) * 320 + 32 * (n % 5);
        wf[n % (PF + 1)] = cat8(tr_read(ad), tr_read(ad + 4 * 320));
      }
#pragma unroll
      for (int n = 0; n < 20; ++n) {
        if (n % 5 == 1 && n < 15) xa[(n / 5 + 1) & 1] = *(LAS bf16x8*)(uintptr_t)(las ^ (32u * (n / 5 + 1)));
        if (n + PF < 20) {
          const unsigned ad = lbs + (16 * ((n + PF) / 5)) * 320 + 32 * ((n + PF) % 5);
          wf[(n + PF) % (PF + 1)] = cat8(tr_read(ad), tr_read(ad + 4 * 320));
        }
        acc[n % 5] = __builtin_amdgcn_mfma_f32_32x32x16_bf16(wf[n % (PF + 1)], xa[(n / 5) & 1], acc[n % 5], 0, 0, 0);
        __builtin_amdgcn_sched_barrier(0);
      }
    }
    STAMP(16);
    __syncthreads();
    __syncthreads();

    const int rl = w * 32 + r32;
    const f32x2 st2 = *(const f32x2*)(smem + K1_STO + rl * 8);
    const float nm = st2[0], rr = st2[1];
    const float kx = rr * -1.44269504f, rr2 = rr * rr;
#pragma unroll
    for (int ti = 0; ti < 5; ++ti) {
      float t[16];
#pragma unroll
      for (int a4 = 0; a4 < 4; ++a4) {
        const f32x4 c4 = *(const f32x4*)(smem + K1_CFO + (ti * 32 + 8 * a4 + 4 * hh) * 4);
#pragma unroll
        for (int r = 0; r < 4; ++r) t[4 * a4 + r] = fmaf(nm, c4[r], acc[ti][4 * a4 + r]);
      }
      if (ti < 2 || !typeB) {
#pragma unroll
        for (int a2 = 0; a2 < 2; ++a2) {
          float hv[4];
#pragma unroll
          for (int r = 0; r < 4; ++r) {
            const float tg = t[4 * a2 + r], tu = t[8 + 4 * a2 + r];
            hv[r] = (tg * tu) * (rr2 * __builtin_amdgcn_rcpf(1.f + __builtin_amdgcn_exp2f(tg * kx)));
          }
          *(u32x2*)(smem + rl * K1_HP + 32 * ti + 16 * a2 + 8 * hh) = u32x2{pack_bf16(hv[0], hv[1]), pack_bf16(hv[2], hv[3])};
        }
      } else {
        const int sw = (rl >> 1) & 3;
#pragma unroll
        for (int a4 = 0; a4 < 4; ++a4) {
          float f[4];
#pragma unroll
          for (int r = 0; r < 4; ++r) {
            const float y = t[4 * a4 + r] * rr;
            if (ti < 4) f[r] = (a4 == 3 && hh) ? 0.f : phi_f(y);
            else        f[r] = (a4 == 0 && !hh) ? (r == 0 ? 1.f : 0.f) : y;
          }
          const u32x2 o = {pack_bf16(f[0], f[1]), pack_bf16(f[2], f[3])};
          if (ti == 2) *(u32x2*)(smem + K1_PQO + rl * 80 + 16 * a4 + 8 * hh) = o;
          else *(u32x2*)(smem + K1_KVO + rl * 128 + (((2 * (ti - 3) + (a4 >> 1)) ^ sw) * 32) + 16 * (a4 & 1) + 8 * hh) = o;
        }
      }
      __builtin_amdgcn_sched_barrier(0);
    }
    STAMP(17);
  }
  __syncthreads();

  char* hb = ws + WS_H + ((size_t)mt * 256 * F_ + fc0) * 2;
  if (!typeB) {
#pragma unroll
    for (int u = 0; u < 3; ++u) {
      const int c = tid + 1024 * u;
      if (u < 2 || c < 2560) {
        const int row = c / 10, ch = c - row * 10;
        *(u32x4*)(hb + (size_t)row * (F_ * 2) + ch * 16) = *(const u32x4*)(smem + row * K1_HP + ch * 16);
      }
    }
  } else {
    if (w < 8) {
#pragma unroll
      for (int u = 0; u < 2; ++u) {
        const int c = tid + 512 * u, row = c >> 2, ch = c & 3;
        *(u32x4*)(hb + (size_t)row * (F_ * 2) + ch * 16) = *(const u32x4*)(smem + row * K1_HP + ch * 16);
        *(u32x4*)(ws + WS_PQ + ((size_t)(mt * 256 + row) * 512 + 32 * jj) * 2 + ch * 16) =
            *(const u32x4*)(smem + K1_PQO + row * 80 + ch * 16);
      }
    } else if (w >= 12) {
      const int dc = hw >> 1, ec = hw & 1;
      const int sig = (2 * g + (q >> 1)) & 3;
      const unsigned lk = lds0 + K1_KVO + (4 * g + q) * 128 + ((dc ^ sig) * 32) + 8 * p;
      const unsigned lv = lds0 + K1_KVO + (4 * g + q) * 128 + (((2 + ec) ^ sig) * 32) + 8 * p;
      f32x4 kv = f32x4{0.f, 0.f, 0.f, 0.f};
#pragma unroll
      for (int ks = 0; ks < 8; ++ks) {
        bf16x8 af = cat8(tr_read(lk + ks * 4096), tr_read(lk + ks * 4096 + 2048));
        bf16x8 bfr = cat8(tr_read(lv + ks * 4096), tr_read(lv + ks * 4096 + 2048));
        kv = mfma16(af, bfr, kv);
      }
      *(f32x4*)(ws + WS_P + ((size_t)(jj * 8 + mt) * 1024 + (16 * ec + m) * 32 + 16 * dc + 4 * g) * 4) = kv;
    }
  }
  STAMP(18); RSTAMP(21);
#undef DIAGOFF
}

constexpr int K2_SLOT = 8192;
constexpr int K2_RSTR = 272;
constexpr int K2_PART = 64 * K2_RSTR;

__global__ __launch_bounds__(512, 2) void k2_kernel(const char* __restrict__ x, const char* __restrict__ wo,
                                                   const char* __restrict__ w2, char* ws, char* __restrict__ out) {
  __shared__ __attribute__((aligned(16))) char smem[20 * K2_SLOT];
  const int tid = threadIdx.x, lane = tid & 63;
  const int w8 = __builtin_amdgcn_readfirstlane(tid >> 6);
  const int w = w8 & 3;
  const int b = blockIdx.x, xcd = b & 7, idx = b >> 3;
  const int mtile = xcd * 4 + (idx & 3), nt = idx >> 2;
  const int R0 = mtile * 64, C0 = nt * 64;
  const int m = lane & 15, g = lane >> 4, q = (lane >> 2) & 3, p = lane & 3;
  const unsigned lds0 = (unsigned)(uintptr_t)(LAS char*)smem;
  const unsigned ring = lds0 + w * 5 * K2_SLOT;
#define DIAGOFF (8u << 20)
  STAMP(0); RSTAMP(20);

  u32x4 xv;
  const unsigned go = (unsigned)(((R0 + (tid >> 3)) * D_ + C0 + (tid & 7) * 8) * 2);
  GLOAD16S(xv, go, x, 0);

  f32x4 acc[4][4], acs[4][4];
  if (w8 >= 4) {
    const int lr = lane >> 3, cbk = lane & 7;
    unsigned aoff[2], qoff[2];
#pragma unroll
    for (int par = 0; par < 2; ++par) {
      const int ch = cbk ^ ((4 * par + (lr >> 1)) & 7);
      aoff[par] = (unsigned)(WS_H + ((size_t)(R0 + lr) * F_ + 448 * w + 8 * ch) * 2);
      qoff[par] = (unsigned)(WS_PQ + ((size_t)(R0 + lr) * 512 + 128 * w + 8 * ch) * 2);
    }
    const int tnd = (cbk >> 1) ^ ((lane >> 4) & 3);
    const unsigned bcol = (unsigned)(C0 + 16 * tnd + 8 * (cbk & 1)) * 2;
    const unsigned boff = (unsigned)(8 * (lr >> 2) + (lr & 3)) * 896 + bcol;
    auto issue = [&](int hu) {
      const unsigned sb = ring + (hu % 5) * K2_SLOT;
      const int un = hu >> 1;
      const bool att = un >= 7;
      const int c = un;
      if (!(hu & 1)) {
        if (!att) {
#pragma unroll
          for (int u = 0; u < 8; ++u) dma16s(aoff[u & 1], ws + (size_t)u * 8 * F_ * 2 + 128 * c, sb + u * 1024);
        } else {
#pragma unroll
          for (int u = 0; u < 8; ++u) dma16s(qoff[u & 1], ws + (size_t)u * 8 * 1024 + 128 * (un - 7), sb + u * 1024);
        }
      } else {
        if (!att) {
          const char* wb = w2 + (size_t)(448 * w + 64 * c) * 896;
#pragma unroll
          for (int u = 0; u < 8; ++u) {
            const int kb = 32 * (u >> 2) + ((u & 1) ? 16 : 0) + ((u & 2) ? 4 : 0);
            dma16s(boff, wb + kb * 896, sb + u * 1024);
          }
        } else {
#pragma unroll
          for (int u = 0; u < 8; ++u) {
            const int e = min(8 * (u & 3) + lr, 27);
            const unsigned vo = (unsigned)(28 * (4 * w + 2 * (un - 7) + (u >> 2)) + e) * 896 + bcol;
            dma16s(vo, wo, sb + u * 1024);
          }
        }
      }
    };
    issue(0); issue(1); issue(2); issue(3); issue(4);
#pragma unroll
    for (int un = 0; un < 9; ++un) {
      if (un == 0) WAITVM(24); else if (un < 8) WAITVM(8); else WAITVM(0);
      BARRIER();
      if (un >= 1) {
        if (2 * un + 3 < 18) issue(2 * un + 3);
        if (2 * un + 4 < 18) issue(2 * un + 4);
      }
    }
  } else {
    if (b < 64 && w == 3) {
      const int head = b >> 2, pos = 8 * (b & 3) + (lane >> 3), d4 = (lane & 7) * 4;
      const float* P = (const float*)(ws + WS_P) + (size_t)head * 8 * 1024 + pos * 32 + d4;
      f32x4 v[8];
#pragma unroll
      for (int t = 0; t < 8; ++t) v[t] = *(const f32x4*)(P + t * 1024);
      f32x4 sacc = v[0];
#pragma unroll
      for (int t = 1; t < 8; ++t) sacc += v[t];
      const int e = pos >= 4 ? pos - 4 : pos + 28;
      unsigned long long o = (unsigned long long)pack_bf16(sacc[0], sacc[1]) |
                             ((unsigned long long)pack_bf16(sacc[2], sacc[3]) << 32);
      __hip_atomic_store((unsigned long long*)(ws + WS_KVT + ((size_t)(head * 32 + e) * 32 + d4) * 2), o,
                         __ATOMIC_RELAXED, __HIP_MEMORY_SCOPE_AGENT);
      WAITVM(0);
      if (lane == 0) __hip_atomic_fetch_add((unsigned*)(ws + WS_CNT), 1u, __ATOMIC_RELAXED, __HIP_MEMORY_SCOPE_AGENT);
    }
    STAMP(1);
    const int sig = (2 * g + (q >> 1)) & 3;
    unsigned lb[4], la[2];
#pragma unroll
    for (int xx = 0; xx < 4; ++xx) lb[xx] = ring + (4 * g + q) * 128 + ((xx ^ sig) * 32) + 8 * p;
#pragma unroll
    for (int ks = 0; ks < 2; ++ks) la[ks] = ring + m * 128 + (((4 * ks + g) ^ ((m >> 1) & 7)) * 16);
#pragma unroll
    for (int i = 0; i < 4; ++i)
#pragma unroll
      for (int t = 0; t < 4; ++t) { acc[i][t] = f32x4{0.f, 0.f, 0.f, 0.f}; acs[i][t] = f32x4{0.f, 0.f, 0.f, 0.f}; }
    u32x4 kvf[4][3];
    unsigned cnt0;
#pragma unroll
    for (int un = 0; un < 9; ++un) {
      const int sa = ((2 * un) % 5) * K2_SLOT, sbb = ((2 * un + 1) % 5) * K2_SLOT;
      BARRIER();
      if (un == 4) {
        asm volatile("global_load_dword %0, %1, %2 sc1" : "=v"(cnt0) : "v"((unsigned)WS_CNT), "s"(ws) : "memory");
      }
      if (un == 5) {
        asm volatile("s_waitcnt vmcnt(0)" : "+v"(cnt0) :: "memory");
        if (__builtin_amdgcn_readfirstlane(cnt0) < 64u) {
          const unsigned* cnt = (const unsigned*)(ws + WS_CNT);
          while (__hip_atomic_load(cnt, __ATOMIC_RELAXED, __HIP_MEMORY_SCOPE_AGENT) < 64u) __builtin_amdgcn_s_sleep(8);
        }
        const unsigned ko = (unsigned)(WS_KVT + ((size_t)(4 * w * 32 + m) * 32 + 8 * g) * 2);
        const unsigned zo = (unsigned)(WS_KVT + ((size_t)(4 * w * 32 + 28) * 32 + 8 * g) * 2);
#pragma unroll
        for (int h4 = 0; h4 < 4; ++h4) {
#pragma unroll
          for (int ec = 0; ec < 2; ++ec)
            asm volatile("global_load_dwordx4 %0, %1, %2 offset:%3 sc1" : "=v"(kvf[h4][ec]) : "v"(ko + (h4 >> 1) * 4096),
                         "s"(ws), "i"(((h4 & 1) * 32 + ec * 16) * 64) : "memory");
          asm volatile("global_load_dwordx4 %0, %1, %2 offset:%3 sc1" : "=v"(kvf[h4][2]) : "v"(zo + (h4 >> 1) * 4096),
                       "s"(ws), "i"((h4 & 1) * 2048) : "memory");
        }
      }
      if (un == 7) {
        asm volatile("s_waitcnt vmcnt(0)" : "+v"(kvf[0][0]), "+v"(kvf[0][1]), "+v"(kvf[1][0]), "+v"(kvf[1][1]), "+v"(kvf[2][0]),
                     "+v"(kvf[2][1]), "+v"(kvf[3][0]), "+v"(kvf[3][1]), "+v"(kvf[0][2]), "+v"(kvf[1][2]), "+v"(kvf[2][2]),
                     "+v"(kvf[3][2]) :: "memory");
        __builtin_amdgcn_sched_barrier(0);
      }
      if (un < 7) {
#pragma unroll
        for (int ks = 0; ks < 2; ++ks) {
          bf16x8 wf[4], af[4];
#pragma unroll
          for (int t = 0; t < 4; ++t) wf[t] = cat8(tr_read(lb[t] + sbb + ks * 4096), tr_read(lb[t] + sbb + ks * 4096 + 2048));
#pragma unroll
          for (int i = 0; i < 4; ++i) af[i] = *(LAS bf16x8*)(uintptr_t)(la[ks] + sa + i * 2048);
#pragma unroll
          for (int t = 0; t < 4; ++t)
#pragma unroll
            for (int i = 0; i < 4; ++i) acc[i][t] = mfma16(wf[t], af[i], acc[i][t]);
        }
      } else {
        f32x4 n0[2][4], n1[2][4], dz[2][4];
#pragma unroll
        for (int ks = 0; ks < 2; ++ks) {
          const int h4 = 2 * (un - 7) + ks;
#pragma unroll
          for (int i = 0; i < 4; ++i) {
            const bf16x8 af = *(LAS bf16x8*)(uintptr_t)(la[ks] + sa + i * 2048);
            dz[ks][i] = mfma16(__builtin_bit_cast(bf16x8, kvf[h4][2]), af, f32x4{0.f, 0.f, 0.f, 0.f});
            n1[ks][i] = mfma16(__builtin_bit_cast(bf16x8, kvf[h4][1]), af, f32x4{0.f, 0.f, 0.f, 0.f});
            n0[ks][i] = mfma16(__builtin_bit_cast(bf16x8, kvf[h4][0]), af, f32x4{0.f, 0.f, 0.f, 0.f});
          }
        }
#pragma unroll
        for (int ks = 0; ks < 2; ++ks) {
          bf16x8 wf[4];
#pragma unroll
          for (int t = 0; t < 4; ++t) wf[t] = cat8(tr_read(lb[t] + sbb + ks * 4096), tr_read(lb[t] + sbb + ks * 4096 + 2048));
#pragma unroll
          for (int i = 0; i < 4; ++i) {
            const float rden = __builtin_amdgcn_rcpf(dz[ks][i][0] + 1e-6f);
            const float rd1 = g == 3 ? 0.f : rden;
            u32x4 ob = {pack_bf16(n0[ks][i][0] * rden, n0[ks][i][1] * rden), pack_bf16(n0[ks][i][2] * rden, n0[ks][i][3] * rden),
                        pack_bf16(n1[ks][i][0] * rd1, n1[ks][i][1] * rd1), pack_bf16(n1[ks][i][2] * rd1, n1[ks][i][3] * rd1)};
            const bf16x8 of = __builtin_bit_cast(bf16x8, ob);
#pragma unroll
            for (int t = 0; t < 4; ++t) acs[i][t] = mfma16(wf[t], of, acs[i][t]);
          }
        }
      }
      __builtin_amdgcn_sched_barrier(0);
      STAMP(un + 2);
    }
  }

  STAMP(18);
  __syncthreads();
  if (w8 < 4) {
#pragma unroll
    for (int i = 0; i < 4; ++i)
#pragma unroll
      for (int t = 0; t < 4; ++t) {
        char* pb = smem + (16 * i + m) * K2_RSTR + (16 * t + 4 * g) * 4;
        *(f32x4*)(pb + (2 * w) * K2_PART) = acc[i][t];
        *(f32x4*)(pb + (2 * w + 1) * K2_PART) = acs[i][t];
      }
  }
  WAITVM(0);
  __syncthreads();
  asm volatile("" : "+v"(xv));
  {
    const int row = tid >> 3, n0 = (tid & 7) * 8;
    unsigned res[4];
#pragma unroll
    for (int v4 = 0; v4 < 2; ++v4) {
      const char* pb = smem + row * K2_RSTR + (n0 + 4 * v4) * 4;
      f32x4 so = *(const f32x4*)(pb), ss = *(const f32x4*)(pb + K2_PART);
#pragma unroll
      for (int u = 1; u < 4; ++u) {
        so += *(const f32x4*)(pb + (2 * u) * K2_PART);
        ss += *(const f32x4*)(pb + (2 * u + 1) * K2_PART);
      }
      float y[4];
#pragma unroll
      for (int r = 0; r < 4; ++r) {
        const unsigned xu = xv[2 * v4 + (r >> 1)];
        const float xf = (r & 1) ? bf_hi(xu) : bf_lo(xu);
        y[r] = xf + ss[r] * silu_f(so[r]);
      }
      res[2 * v4] = pack_bf16(y[0], y[1]);
      res[2 * v4 + 1] = pack_bf16(y[2], y[3]);
    }
    *(u32x4*)(out + go) = u32x4{res[0], res[1], res[2], res[3]};
  }
  STAMP(19); RSTAMP(21);
#undef DIAGOFF
}

extern "C" void kernel_launch(void* const* d_in, const int* in_sizes, int n_in,
                              void* d_out, int out_size, void* d_ws, size_t ws_size,
                              hipStream_t stream) {
  const char* x    = (const char*)d_in[0];
  const char* wqkv = (const char*)d_in[1];
  const char* wo   = (const char*)d_in[2];
  const char* w1   = (const char*)d_in[4];
  const char* w3   = (const char*)d_in[5];
  const char* w2   = (const char*)d_in[6];
  k1_kernel<<<256, 1024, 0, stream>>>(x, wqkv, w1, w3, (char*)d_ws);
  k2_kernel<<<224, 512, 0, stream>>>(x, wo, w2, (char*)d_ws, (char*)d_out);
}
```

```cpp
#include <hip/hip_runtime.h>
#include <hip/hip_bf16.h>
#include <stdint.h>

typedef short    bf16x8 __attribute__((ext_vector_type(8)));
typedef short    bf16x4 __attribute__((ext_vector_type(4)));
typedef float    f32x4  __attribute__((ext_vector_type(4)));
typedef float    f32x2  __attribute__((ext_vector_type(2)));
typedef float    f32x16 __attribute__((ext_vector_type(16)));
typedef unsigned u32x4  __attribute__((ext_vector_type(4)));
typedef unsigned u32x2  __attribute__((ext_vector_type(2)));
typedef __bf16   bf2_t  __attribute__((ext_vector_type(2)));
#define LAS __attribute__((address_space(3)))

constexpr int S_ = 2048, D_ = 448, F_ = 1792;
constexpr size_t WS_H   = 0;
constexpr size_t WS_PQ  = WS_H + (size_t)S_ * F_ * 2;
constexpr size_t WS_P   = WS_PQ + (size_t)S_ * 512 * 2;
constexpr size_t WS_KVT = WS_P + (size_t)16 * 16 * 1024 * 4;
constexpr size_t WS_CNT = WS_KVT + (size_t)16 * 1024 * 2;

__device__ __forceinline__ unsigned pack_bf16(float lo, float hi) {
  f32x2 v = {lo, hi};
  bf2_t r = __builtin_convertvector(v, bf2_t);
  return __builtin_bit_cast(unsigned, r);
}
__device__ __forceinline__ float bf_lo(unsigned u) { return __uint_as_float(u << 16); }
__device__ __forceinline__ float bf_hi(unsigned u) { return __uint_as_float(u & 0xffff0000u); }
__device__ __forceinline__ float rbf(float f) { return bf_lo(pack_bf16(f, 0.f) & 0xffffu); }
__device__ __forceinline__ float silu_f(float v) { return v * __builtin_amdgcn_rcpf(1.f + __expf(-v)); }
__device__ __forceinline__ float phi_f(float t) { return (t > 0.f ? t + 1.f : __expf(t)) + 1e-6f; }
__device__ __forceinline__ f32x4 mfma16(bf16x8 a, bf16x8 b, f32x4 c) {
  return __builtin_amdgcn_mfma_f32_16x16x32_bf16(a, b, c, 0, 0, 0);
}
__device__ __forceinline__ bf16x4 tr_read(unsigned lds_addr) {
  return __builtin_amdgcn_ds_read_tr16_b64_v4i16((LAS bf16x4*)(uintptr_t)lds_addr);
}
__device__ __forceinline__ bf16x8 cat8(bf16x4 lo, bf16x4 hi) {
  return __builtin_shufflevector(lo, hi, 0, 1, 2, 3, 4, 5, 6, 7);
}
__device__ __forceinline__ void dma16(const void* gsrc, unsigned lds_dst) {
  unsigned keep;
  asm volatile("s_mov_b32 %0, m0\n\ts_mov_b32 m0, %2\n\ts_nop 0\n\tglobal_load_lds_dwordx4 %1, off\n\ts_mov_b32 m0, %0"
               : "=&s"(keep) : "v"(gsrc), "s"(lds_dst) : "memory");
}
__device__ __forceinline__ void dma16s(unsigned voff, const void* sbase, unsigned lds_dst) {
  unsigned keep;
  asm volatile("s_mov_b32 %0, m0\n\ts_mov_b32 m0, %3\n\ts_nop 0\n\tglobal_load_lds_dwordx4 %1, %2\n\ts_mov_b32 m0, %0"
               : "=&s"(keep) : "v"(voff), "s"(sbase), "s"(lds_dst) : "memory");
}
#define GLOAD16(dst, ptr, off) \
  asm volatile("global_load_dwordx4 %0, %1, off offset:%2" : "=v"(dst) : "v"(ptr), "i"(off) : "memory")
#define GLOAD16S(dst, voff, sbase, off) \
  asm volatile("global_load_dwordx4 %0, %1, %2 offset:%3" : "=v"(dst) : "v"(voff), "s"(sbase), "i"(off) : "memory")
#define WAITVM(n) asm volatile("s_waitcnt vmcnt(" #n ")" ::: "memory")
#define BARRIER() asm volatile("s_barrier" ::: "memory")
#define STAMP(i)
#define RSTAMP(i)

#define K1_ST(ptr, v) (*(ptr) = (v))
constexpr int K1_SLOT = 53248;
constexpr int K1_BOFF = 32768;
constexpr int K1_HP = 176;
constexpr int K1_PQO = 49152;
constexpr int K1_KVO = 73728;
constexpr int K1_CSO = 110592;
constexpr int K1_STO = 114688;
constexpr int K1_CFO = 118784;

__global__ __launch_bounds__(1024, 4) void k1_kernel(const char* __restrict__ x, const char* __restrict__ wqkv,
                                                    const char* __restrict__ w1, const char* __restrict__ w3,
                                                    char* __restrict__ ws) {
  __shared__ __attribute__((aligned(16))) char smem[3 * K1_SLOT];
  const int tid = threadIdx.x, lane = tid & 63;
  const int w = __builtin_amdgcn_readfirstlane(tid >> 6);
  const int b = blockIdx.x, xcd = b & 7, idx = b >> 3;
  const int mt = 4 * (xcd & 1) + (idx & 3), gidx = 8 * (xcd >> 1) + (idx >> 2);
  const int jj = gidx >> 1, typeB = gidx & 1;
  const int fc0 = 112 * jj + (typeB ? 80 : 0);
  const int m = lane & 15, g = lane >> 4, q = (lane >> 2) & 3, p = lane & 3;
  const int r32 = lane & 31, hh = lane >> 5;
  const int hw = w & 3;
  const unsigned lds0 = (unsigned)(uintptr_t)(LAS char*)smem;

  if (b == 0 && tid == 0) *(unsigned*)(ws + WS_CNT) = 0u;
#define DIAGOFF 0
  STAMP(0); RSTAMP(20);

  if (w >= 12) {
    const unsigned lrow = lds0 + (64 * hw + m) * 128 + ((g ^ ((m >> 1) & 7)) * 16);
    const bf16x8 ones8 = {0x3f80, 0x3f80, 0x3f80, 0x3f80, 0x3f80, 0x3f80, 0x3f80, 0x3f80};
    const unsigned lbw = lds0 + K1_BOFF + (32 * (hw & 1) + 4 * g + q) * 320 + (hw >> 1) * 160 + 8 * p;
    f32x4 cs[5];
#pragma unroll
    for (int t = 0; t < 5; ++t) cs[t] = f32x4{0.f, 0.f, 0.f, 0.f};
    f32x4 g2[4], g1[4];
#pragma unroll
    for (int i = 0; i < 4; ++i) { g2[i] = f32x4{0.f, 0.f, 0.f, 0.f}; g1[i] = f32x4{0.f, 0.f, 0.f, 0.f}; }
#pragma unroll
    for (int st = 0; st < 7; ++st) {
      BARRIER();
      unsigned lr2 = lrow + (st % 3) * K1_SLOT, lb2 = lbw + (st % 3) * K1_SLOT;
      asm volatile("" : "+v"(lr2), "+v"(lb2));
#pragma unroll
      for (int ti = 0; ti < 5; ++ti)
        cs[ti] = mfma16(ones8, cat8(tr_read(lb2 + 32 * ti), tr_read(lb2 + 32 * ti + 16 * 320)), cs[ti]);
#pragma unroll
      for (int ks = 0; ks < 2; ++ks)
#pragma unroll
        for (int i = 0; i < 4; ++i) {
          const bf16x8 af = *(LAS bf16x8*)(uintptr_t)((lr2 ^ (64u * ks)) + i * 2048);
          g2[i] = mfma16(af, af, g2[i]);
          g1[i] = mfma16(ones8, af, g1[i]);
        }
    }
    if (g == 0) {
#pragma unroll
      for (int ti = 0; ti < 5; ++ti)
        *(float*)(smem + K1_CSO + (ti * 32 + 16 * (hw >> 1) + m) * 8 + (hw & 1) * 4) = cs[ti][0];
    }
    if (g == (m >> 2)) {
#pragma unroll
      for (int i = 0; i < 4; ++i) {
        const int r = m & 3;
        const float s2 = r == 0 ? g2[i][0] : (r == 1 ? g2[i][1] : (r == 2 ? g2[i][2] : g2[i][3]));
        const float mean = g1[i][0] * (1.f / 448.f);
        const float var = fmaxf(s2 * (1.f / 448.f) - mean * mean, 0.f);
        *(f32x2*)(smem + K1_STO + (64 * hw + 16 * i + m) * 8) =
            f32x2{-rbf(mean), __builtin_amdgcn_rcpf(rbf(__builtin_amdgcn_sqrtf(rbf(rbf(var) + 1e-5f))))};
      }
    }
    __syncthreads();
    if (tid - 768 < 160) {
      const f32x2 c2 = *(const f32x2*)(smem + K1_CSO + (tid - 768) * 8);
      *(float*)(smem + K1_CFO + (tid - 768) * 4) = c2[0] + c2[1];
    }
    __syncthreads();
  } else if (w >= 8) {
    const char* xb = x + (size_t)mt * 256 * 896;
    unsigned aoff[2];
#pragma unroll
    for (int par = 0; par < 2; ++par)
      aoff[par] = (unsigned)(64 * hw + (lane >> 3)) * 896 + (((lane & 7) ^ ((4 * par + (lane >> 4)) & 7)) * 16);
#pragma unroll
    for (int u = 0; u < 8; ++u) dma16s(aoff[u & 1], xb + 8 * 896 * u, lds0 + (8 * hw + u) * 1024);
    const char* src[5];
    unsigned inc[5];
#pragma unroll
    for (int u = 0; u < 5; ++u) {
      const int o = 1024 * (5 * hw + u) + 16 * lane;
      const int r = o / 320, cb = o - r * 320;
      const int P = cb >> 5;
      const int Pm = P >= 5 ? P - 5 : P, hi = P >= 5 ? 1 : 0;
      const bool isq = typeB && Pm >= 2;
      const char* base = isq ? wqkv : (hi ? w3 : w1);
      const int col = isq ? (Pm == 2 ? 0 : (Pm == 3 ? 448 : 892)) + 28 * jj + 16 * hi : fc0 + 16 * Pm;
      src[u] = base + (size_t)r * (isq ? 2688 : 3584) + col * 2 + (cb & 16);
      inc[u] = isq ? 172032u : 229376u;
    }
    auto issueB = [&](int st) {
#pragma unroll
      for (int u = 0; u < 5; ++u) {
        dma16(src[u], lds0 + (st % 3) * K1_SLOT + K1_BOFF + (5 * hw + u) * 1024);
        src[u] += inc[u];
      }
    };
    auto issueA = [&](int st) {
#pragma unroll
      for (int u = 0; u < 8; ++u)
        dma16s(aoff[u & 1], xb + 8 * 896 * u + 128 * st, lds0 + (st % 3) * K1_SLOT + (8 * hw + u) * 1024);
    };
    issueB(0); issueA(1); issueB(1);
#pragma unroll
    for (int st = 0; st < 7; ++st) {
      if (st < 6) WAITVM(13); else WAITVM(0);
      BARRIER();
      if (st + 2 < 7) { issueA(st + 2); issueB(st + 2); }
    }
    __syncthreads();
    __syncthreads();
  } else {
    const unsigned lb = lds0 + K1_BOFF + (8 * (g >> 1) + q) * 320 + (g & 1) * 160 + 8 * p;
    const unsigned la = lds0 + (w * 32 + r32) * 128 + ((hh ^ ((r32 >> 1) & 7)) * 16);
    f32x16 acc[5];
#pragma unroll
    for (int t = 0; t < 5; ++t)
#pragma unroll
      for (int e = 0; e < 16; ++e) acc[t][e] = 0.f;
#pragma unroll
    for (int st = 0; st < 7; ++st) {
      STAMP(2 + 2 * st);
      BARRIER();
      STAMP(3 + 2 * st);
      constexpr int PF = 4;
      unsigned lbs = lb + (st % 3) * K1_SLOT, las = la + (st % 3) * K1_SLOT;
      asm volatile("" : "+v"(lbs), "+v"(las));
      bf16x8 wf[PF + 1], xa[2];
      xa[0] = *(LAS bf16x8*)(uintptr_t)(las);
#pragma unroll
      for (int n = 0; n < PF; ++n) {
        const unsigned ad = lbs + (16 * (n / 5)) * 320 + 32 * (n % 5);
        wf[n % (PF + 1)] = cat8(tr_read(ad), tr_read(ad + 4 * 320));
      }
#pragma unroll
      for (int n = 0; n < 20; ++n) {
        if (n % 5 == 1 && n < 15) xa[(n / 5 + 1) & 1] = *(LAS bf16x8*)(uintptr_t)(las ^ (32u * (n / 5 + 1)));
        if (n + PF < 20) {
          const unsigned ad = lbs + (16 * ((n + PF) / 5)) * 320 + 32 * ((n + PF) % 5);
          wf[(n + PF) % (PF + 1)] = cat8(tr_read(ad), tr_read(ad + 4 * 320));
        }
        acc[n % 5] = __builtin_amdgcn_mfma_f32_32x32x16_bf16(wf[n % (PF + 1)], xa[(n / 5) & 1], acc[n % 5], 0, 0, 0);
        __builtin_amdgcn_sched_barrier(0);
      }
    }
    STAMP(16);
    __syncthreads();
    __syncthreads();

    const int rl = w * 32 + r32;
    const f32x2 st2 = *(const f32x2*)(smem + K1_STO + rl * 8);
    const float nm = st2[0], rr = st2[1];
    const float kx = rr * -1.44269504f, rr2 = rr * rr;
#pragma unroll
    for (int ti = 0; ti < 5; ++ti) {
      float t[16];
#pragma unroll
      for (int a4 = 0; a4 < 4; ++a4) {
        const f32x4 c4 = *(const f32x4*)(smem + K1_CFO + (ti * 32 + 8 * a4 + 4 * hh) * 4);
#pragma unroll
        for (int r = 0; r < 4; ++r) t[4 * a4 + r] = fmaf(nm, c4[r], acc[ti][4 * a4 + r]);
      }
      if (ti < 2 || !typeB) {
#pragma unroll
        for (int a2 = 0; a2 < 2; ++a2) {
          float hv[4];
#pragma unroll
          for (int r = 0; r < 4; ++r) {
            const float tg = t[4 * a2 + r], tu = t[8 + 4 * a2 + r];
            hv[r] = (tg * tu) * (rr2 * __builtin_amdgcn_rcpf(1.f + __builtin_amdgcn_exp2f(tg * kx)));
          }
          *(u32x2*)(smem + rl * K1_HP + 32 * ti + 16 * a2 + 8 * hh) = u32x2{pack_bf16(hv[0], hv[1]), pack_bf16(hv[2], hv[3])};
        }
      } else {
        const int sw = (rl >> 1) & 3;
#pragma unroll
        for (int a4 = 0; a4 < 4; ++a4) {
          float f[4];
#pragma unroll
          for (int r = 0; r < 4; ++r) {
            const float y = t[4 * a4 + r] * rr;
            if (ti < 4) f[r] = (a4 == 3 && hh) ? 0.f : phi_f(y);
            else        f[r] = (a4 == 0 && !hh) ? (r == 0 ? 1.f : 0.f) : y;
          }
          const u32x2 o = {pack_bf16(f[0], f[1]), pack_bf16(f[2], f[3])};
          if (ti == 2) *(u32x2*)(smem + K1_PQO + rl * 80 + 16 * a4 + 8 * hh) = o;
          else *(u32x2*)(smem + K1_KVO + rl * 128 + (((2 * (ti - 3) + (a4 >> 1)) ^ sw) * 32) + 16 * (a4 & 1) + 8 * hh) = o;
        }
      }
      __builtin_amdgcn_sched_barrier(0);
    }
    STAMP(17);
  }
  __syncthreads();

  char* hb = ws + WS_H + ((size_t)mt * 256 * F_ + fc0) * 2;
  if (!typeB) {
#pragma unroll
    for (int u = 0; u < 3; ++u) {
      const int c = tid + 1024 * u;
      if (u < 2 || c < 2560) {
        const int row = c / 10, ch = c - row * 10;
        K1_ST((u32x4*)(hb + (size_t)row * (F_ * 2) + ch * 16), *(const u32x4*)(smem + row * K1_HP + ch * 16));
      }
    }
  } else {
    if (w < 8) {
#pragma unroll
      for (int u = 0; u < 2; ++u) {
        const int c = tid + 512 * u, row = c >> 2, ch = c & 3;
        K1_ST((u32x4*)(hb + (size_t)row * (F_ * 2) + ch * 16), *(const u32x4*)(smem + row * K1_HP + ch * 16));
        K1_ST((u32x4*)(ws + WS_PQ + ((size_t)(mt * 256 + row) * 512 + 32 * jj) * 2 + ch * 16),
              *(const u32x4*)(smem + K1_PQO + row * 80 + ch * 16));
      }
    } else if (w >= 12) {
      const int dc = hw >> 1, ec = hw & 1;
      const int sig = (2 * g + (q >> 1)) & 3;
      const unsigned lk = lds0 + K1_KVO + (4 * g + q) * 128 + ((dc ^ sig) * 32) + 8 * p;
      const unsigned lv = lds0 + K1_KVO + (4 * g + q) * 128 + (((2 + ec) ^ sig) * 32) + 8 * p;
      f32x4 kv = f32x4{0.f, 0.f, 0.f, 0.f};
#pragma unroll
      for (int ks = 0; ks < 8; ++ks) {
        bf16x8 af = cat8(tr_read(lk + ks * 4096), tr_read(lk + ks * 4096 + 2048));
        bf16x8 bfr = cat8(tr_read(lv + ks * 4096), tr_read(lv + ks * 4096 + 2048));
        kv = mfma16(af, bfr, kv);
      }
      *(f32x4*)(ws + WS_P + ((size_t)(jj * 8 + mt) * 1024 + (16 * ec + m) * 32 + 16 * dc + 4 * g) * 4) = kv;
    }
  }
  STAMP(18); RSTAMP(21);
#undef DIAGOFF
}

constexpr int K2_SLOT = 8192;
constexpr int K2_RSTR = 272;
constexpr int K2_PART = 64 * K2_RSTR;

__global__ __launch_bounds__(512, 2) void k2_kernel(const char* __restrict__ x, const char* __restrict__ wo,
                                                   const char* __restrict__ w2, char* ws, char* __restrict__ out) {
  __shared__ __attribute__((aligned(16))) char smem[20 * K2_SLOT];
  const int tid = threadIdx.x, lane = tid & 63;
  const int w8 = __builtin_amdgcn_readfirstlane(tid >> 6);
  const int w = w8 & 3;
  const int b = blockIdx.x, xcd = b & 7, idx = b >> 3;
  const int mtile = xcd * 4 + (idx & 3), nt = idx >> 2;
  const int R0 = mtile * 64, C0 = nt * 64;
  const int m = lane & 15, g = lane >> 4, q = (lane >> 2) & 3, p = lane & 3;
  const unsigned lds0 = (unsigned)(uintptr_t)(LAS char*)smem;
  const unsigned ring = lds0 + w * 5 * K2_SLOT;
#define DIAGOFF (8u << 20)
  STAMP(0); RSTAMP(20);

  u32x4 xv;
  const unsigned go = (unsigned)(((R0 + (tid >> 3)) * D_ + C0 + (tid & 7) * 8) * 2);
  GLOAD16S(xv, go, x, 0);

  f32x4 acc[4][4], acs[4][4];
  if (w8 >= 4) {
    const int lr = lane >> 3, cbk = lane & 7;
    unsigned aoff[2], qoff[2];
#pragma unroll
    for (int par = 0; par < 2; ++par) {
      const int ch = cbk ^ ((4 * par + (lr >> 1)) & 7);
      aoff[par] = (unsigned)(WS_H + ((size_t)(R0 + lr) * F_ + 448 * w + 8 * ch) * 2);
      qoff[par] = (unsigned)(WS_PQ + ((size_t)(R0 + lr) * 512 + 128 * w + 8 * ch) * 2);
    }
    const int tnd = (cbk >> 1) ^ ((lane >> 4) & 3);
    const unsigned bcol = (unsigned)(C0 + 16 * tnd + 8 * (cbk & 1)) * 2;
    const unsigned boff = (unsigned)(8 * (lr >> 2) + (lr & 3)) * 896 + bcol;
    auto issue = [&](int hu) {
      const unsigned sb = ring + (hu % 5) * K2_SLOT;
      const int un = hu >> 1;
      const bool att = un >= 7;
      const int c = un;
      if (!(hu & 1)) {
        if (!att) {
#pragma unroll
          for (int u = 0; u < 8; ++u) dma16s(aoff[u & 1], ws + (size_t)u * 8 * F_ * 2 + 128 * c, sb + u * 1024);
        } else {
#pragma unroll
          for (int u = 0; u < 8; ++u) dma16s(qoff[u & 1], ws + (size_t)u * 8 * 1024 + 128 * (un - 7), sb + u * 1024);
        }
      } else {
        if (!att) {
          const char* wb = w2 + (size_t)(448 * w + 64 * c) * 896;
#pragma unroll
          for (int u = 0; u < 8; ++u) {
            const int kb = 32 * (u >> 2) + ((u & 1) ? 16 : 0) + ((u & 2) ? 4 : 0);
            dma16s(boff, wb + kb * 896, sb + u * 1024);
          }
        } else {
#pragma unroll
          for (int u = 0; u < 8; ++u) {
            const int e = min(8 * (u & 3) + lr, 27);
            const unsigned vo = (unsigned)(28 * (4 * w + 2 * (un - 7) + (u >> 2)) + e) * 896 + bcol;
            dma16s(vo, wo, sb + u * 1024);
          }
        }
      }
    };
    issue(0); issue(1); issue(2); issue(3); issue(4);
#pragma unroll
    for (int un = 0; un < 9; ++un) {
      if (un == 0) WAITVM(24); else if (un < 8) WAITVM(8); else WAITVM(0);
      BARRIER();
      if (un >= 1) {
        if (2 * un + 3 < 18) issue(2 * un + 3);
        if (2 * un + 4 < 18) issue(2 * un + 4);
      }
    }
  } else {
    if (b < 64 && w == 3) {
      const int head = b >> 2, pos = 8 * (b & 3) + (lane >> 3), d4 = (lane & 7) * 4;
      const float* P = (const float*)(ws + WS_P) + (size_t)head * 8 * 1024 + pos * 32 + d4;
      f32x4 v[8];
#pragma unroll
      for (int t = 0; t < 8; ++t) v[t] = *(const f32x4*)(P + t * 1024);
      f32x4 sacc = v[0];
#pragma unroll
      for (int t = 1; t < 8; ++t) sacc += v[t];
      const int e = pos >= 4 ? pos - 4 : pos + 28;
      unsigned long long o = (unsigned long long)pack_bf16(sacc[0], sacc[1]) |
                             ((unsigned long long)pack_bf16(sacc[2], sacc[3]) << 32);
      __hip_atomic_store((unsigned long long*)(ws + WS_KVT + ((size_t)(head * 32 + e) * 32 + d4) * 2), o,
                         __ATOMIC_RELAXED, __HIP_MEMORY_SCOPE_AGENT);
      WAITVM(0);
      if (lane == 0) __hip_atomic_fetch_add((unsigned*)(ws + WS_CNT), 1u, __ATOMIC_RELAXED, __HIP_MEMORY_SCOPE_AGENT);
    }
    STAMP(1);
    const int sig = (2 * g + (q >> 1)) & 3;
    unsigned lb[4], la[2];
#pragma unroll
    for (int xx = 0; xx < 4; ++xx) lb[xx] = ring + (4 * g + q) * 128 + ((xx ^ sig) * 32) + 8 * p;
#pragma unroll
    for (int ks = 0; ks < 2; ++ks) la[ks] = ring + m * 128 + (((4 * ks + g) ^ ((m >> 1) & 7)) * 16);
#pragma unroll
    for (int i = 0; i < 4; ++i)
#pragma unroll
      for (int t = 0; t < 4; ++t) { acc[i][t] = f32x4{0.f, 0.f, 0.f, 0.f}; acs[i][t] = f32x4{0.f, 0.f, 0.f, 0.f}; }
    u32x4 kvf[4][3];
    unsigned cnt0;
#pragma unroll
    for (int un = 0; un < 9; ++un) {
      const int sa = ((2 * un) % 5) * K2_SLOT, sbb = ((2 * un + 1) % 5) * K2_SLOT;
      BARRIER();
      if (un == 5) {
        asm volatile("global_load_dword %0, %1, %2 sc1" : "=v"(cnt0) : "v"((unsigned)WS_CNT), "s"(ws) : "memory");
      }
      if (un == 6) {
        asm volatile("s_waitcnt vmcnt(0)" : "+v"(cnt0) :: "memory");
        if (__builtin_amdgcn_readfirstlane(cnt0) < 64u) {
          const unsigned* cnt = (const unsigned*)(ws + WS_CNT);
          while (__hip_atomic_load(cnt, __ATOMIC_RELAXED, __HIP_MEMORY_SCOPE_AGENT) < 64u) __builtin_amdgcn_s_sleep(8);
        }
        const unsigned ko = (unsigned)(WS_KVT + ((size_t)(4 * w * 32 + m) * 32 + 8 * g) * 2);
        const unsigned zo = (unsigned)(WS_KVT + ((size_t)(4 * w * 32 + 28) * 32 + 8 * g) * 2);
#pragma unroll
        for (int h4 = 0; h4 < 4; ++h4) {
#pragma unroll
          for (int ec = 0; ec < 2; ++ec)
            asm volatile("global_load_dwordx4 %0, %1, %2 offset:%3 sc1" : "=v"(kvf[h4][ec]) : "v"(ko + (h4 >> 1) * 4096),
                         "s"(ws), "i"(((h4 & 1) * 32 + ec * 16) * 64) : "memory");
          asm volatile("global_load_dwordx4 %0, %1, %2 offset:%3 sc1" : "=v"(kvf[h4][2]) : "v"(zo + (h4 >> 1) * 4096),
                       "s"(ws), "i"((h4 & 1) * 2048) : "memory");
        }
      }
      if (un == 7) {
        asm volatile("s_waitcnt vmcnt(0)" : "+v"(kvf[0][0]), "+v"(kvf[0][1]), "+v"(kvf[1][0]), "+v"(kvf[1][1]), "+v"(kvf[2][0]),
                     "+v"(kvf[2][1]), "+v"(kvf[3][0]), "+v"(kvf[3][1]), "+v"(kvf[0][2]), "+v"(kvf[1][2]), "+v"(kvf[2][2]),
                     "+v"(kvf[3][2]) :: "memory");
        __builtin_amdgcn_sched_barrier(0);
      }
      if (un < 7) {
#pragma unroll
        for (int ks = 0; ks < 2; ++ks) {
          bf16x8 wf[4], af[4];
#pragma unroll
          for (int t = 0; t < 4; ++t) wf[t] = cat8(tr_read(lb[t] + sbb + ks * 4096), tr_read(lb[t] + sbb + ks * 4096 + 2048));
#pragma unroll
          for (int i = 0; i < 4; ++i) af[i] = *(LAS bf16x8*)(uintptr_t)(la[ks] + sa + i * 2048);
#pragma unroll
          for (int t = 0; t < 4; ++t)
#pragma unroll
            for (int i = 0; i < 4; ++i) acc[i][t] = mfma16(wf[t], af[i], acc[i][t]);
        }
      } else {
        f32x4 n0[2][4], n1[2][4], dz[2][4];
#pragma unroll
        for (int ks = 0; ks < 2; ++ks) {
          const int h4 = 2 * (un - 7) + ks;
#pragma unroll
          for (int i = 0; i < 4; ++i) {
            const bf16x8 af = *(LAS bf16x8*)(uintptr_t)(la[ks] + sa + i * 2048);
            dz[ks][i] = mfma16(__builtin_bit_cast(bf16x8, kvf[h4][2]), af, f32x4{0.f, 0.f, 0.f, 0.f});
            n1[ks][i] = mfma16(__builtin_bit_cast(bf16x8, kvf[h4][1]), af, f32x4{0.f, 0.f, 0.f, 0.f});
            n0[ks][i] = mfma16(__builtin_bit_cast(bf16x8, kvf[h4][0]), af, f32x4{0.f, 0.f, 0.f, 0.f});
          }
        }
#pragma unroll
        for (int ks = 0; ks < 2; ++ks) {
          bf16x8 wf[4];
#pragma unroll
          for (int t = 0; t < 4; ++t) wf[t] = cat8(tr_read(lb[t] + sbb + ks * 4096), tr_read(lb[t] + sbb + ks * 4096 + 2048));
#pragma unroll
          for (int i = 0; i < 4; ++i) {
            const float rden = __builtin_amdgcn_rcpf(dz[ks][i][0] + 1e-6f);
            const float rd1 = g == 3 ? 0.f : rden;
            u32x4 ob = {pack_bf16(n0[ks][i][0] * rden, n0[ks][i][1] * rden), pack_bf16(n0[ks][i][2] * rden, n0[ks][i][3] * rden),
                        pack_bf16(n1[ks][i][0] * rd1, n1[ks][i][1] * rd1), pack_bf16(n1[ks][i][2] * rd1, n1[ks][i][3] * rd1)};
            const bf16x8 of = __builtin_bit_cast(bf16x8, ob);
#pragma unroll
            for (int t = 0; t < 4; ++t) acs[i][t] = mfma16(wf[t], of, acs[i][t]);
          }
        }
      }
      __builtin_amdgcn_sched_barrier(0);
      STAMP(un + 2);
    }
  }

  STAMP(18);
  __syncthreads();
  if (w8 < 4) {
#pragma unroll
    for (int i = 0; i < 4; ++i)
#pragma unroll
      for (int t = 0; t < 4; ++t) {
        char* pb = smem + (16 * i + m) * K2_RSTR + (16 * t + 4 * g) * 4;
        *(f32x4*)(pb + (2 * w) * K2_PART) = acc[i][t];
        *(f32x4*)(pb + (2 * w + 1) * K2_PART) = acs[i][t];
      }
  }
  WAITVM(0);
  __syncthreads();
  asm volatile("" : "+v"(xv));
  {
    const int row = tid >> 3, n0 = (tid & 7) * 8;
    unsigned res[4];
#pragma unroll
    for (int v4 = 0; v4 < 2; ++v4) {
      const char* pb = smem + row * K2_RSTR + (n0 + 4 * v4) * 4;
      f32x4 so = *(const f32x4*)(pb), ss = *(const f32x4*)(pb + K2_PART);
#pragma unroll
      for (int u = 1; u < 4; ++u) {
        so += *(const f32x4*)(pb + (2 * u) * K2_PART);
        ss += *(const f32x4*)(pb + (2 * u + 1) * K2_PART);
      }
      float y[4];
#pragma unroll
      for (int r = 0; r < 4; ++r) {
        const unsigned xu = xv[2 * v4 + (r >> 1)];
        const float xf = (r & 1) ? bf_hi(xu) : bf_lo(xu);
        y[r] = xf + ss[r] * silu_f(so[r]);
      }
      res[2 * v4] = pack_bf16(y[0], y[1]);
      res[2 * v4 + 1] = pack_bf16(y[2], y[3]);
    }
    *(u32x4*)(out + go) = u32x4{res[0], res[1], res[2], res[3]};
  }
  STAMP(19); RSTAMP(21);
#undef DIAGOFF
}

extern "C" void kernel_launch(void* const* d_in, const int* in_sizes, int n_in,
                              void* d_out, int out_size, void* d_ws, size_t ws_size,
                              hipStream_t stream) {
  const char* x    = (const char*)d_in[0];
  const char* wqkv = (const char*)d_in[1];
  const char* wo   = (const char*)d_in[2];
  const char* w1   = (const char*)d_in[4];
  const char* w3   = (const char*)d_in[5];
  const char* w2   = (const char*)d_in[6];
  k1_kernel<<<256, 1024, 0, stream>>>(x, wqkv, w1, w3, (char*)d_ws);
  k2_kernel<<<224, 512, 0, stream>>>(x, wo, w2, (char*)d_ws, (char*)d_out);
}
```

```cpp
#include <hip/hip_runtime.h>
#include <hip/hip_bf16.h>
#include <stdint.h>
#define K1_WARM

typedef short    bf16x8 __attribute__((ext_vector_type(8)));
typedef short    bf16x4 __attribute__((ext_vector_type(4)));
typedef float    f32x4  __attribute__((ext_vector_type(4)));
typedef float    f32x2  __attribute__((ext_vector_type(2)));
typedef float    f32x16 __attribute__((ext_vector_type(16)));
typedef unsigned u32x4  __attribute__((ext_vector_type(4)));
typedef unsigned u32x2  __attribute__((ext_vector_type(2)));
typedef __bf16   bf2_t  __attribute__((ext_vector_type(2)));
#define LAS __attribute__((address_space(3)))

constexpr int S_ = 2048, D_ = 448, F_ = 1792;
constexpr size_t WS_H   = 0;
constexpr size_t WS_PQ  = WS_H + (size_t)S_ * F_ * 2;
constexpr size_t WS_P   = WS_PQ + (size_t)S_ * 512 * 2;
constexpr size_t WS_KVT = WS_P + (size_t)16 * 16 * 1024 * 4;
constexpr size_t WS_CNT = WS_KVT + (size_t)16 * 1024 * 2;

__device__ __forceinline__ unsigned pack_bf16(float lo, float hi) {
  f32x2 v = {lo, hi};
  bf2_t r = __builtin_convertvector(v, bf2_t);
  return __builtin_bit_cast(unsigned, r);
}
__device__ __forceinline__ float bf_lo(unsigned u) { return __uint_as_float(u << 16); }
__device__ __forceinline__ float bf_hi(unsigned u) { return __uint_as_float(u & 0xffff0000u); }
__device__ __forceinline__ float rbf(float f) { return bf_lo(pack_bf16(f, 0.f) & 0xffffu); }
__device__ __forceinline__ float silu_f(float v) { return v * __builtin_amdgcn_rcpf(1.f + __expf(-v)); }
__device__ __forceinline__ float phi_f(float t) { return (t > 0.f ? t + 1.f : __expf(t)) + 1e-6f; }
__device__ __forceinline__ f32x4 mfma16(bf16x8 a, bf16x8 b, f32x4 c) {
  return __builtin_amdgcn_mfma_f32_16x16x32_bf16(a, b, c, 0, 0, 0);
}
__device__ __forceinline__ bf16x4 tr_read(unsigned lds_addr) {
  return __builtin_amdgcn_ds_read_tr16_b64_v4i16((LAS bf16x4*)(uintptr_t)lds_addr);
}
__device__ __forceinline__ bf16x8 cat8(bf16x4 lo, bf16x4 hi) {
  return __builtin_shufflevector(lo, hi, 0, 1, 2, 3, 4, 5, 6, 7);
}
__device__ __forceinline__ void dma16(const void* gsrc, unsigned lds_dst) {
  unsigned keep;
  asm volatile("s_mov_b32 %0, m0\n\ts_mov_b32 m0, %2\n\ts_nop 0\n\tglobal_load_lds_dwordx4 %1, off\n\ts_mov_b32 m0, %0"
               : "=&s"(keep) : "v"(gsrc), "s"(lds_dst) : "memory");
}
__device__ __forceinline__ void dma16s(unsigned voff, const void* sbase, unsigned lds_dst) {
  unsigned keep;
  asm volatile("s_mov_b32 %0, m0\n\ts_mov_b32 m0, %3\n\ts_nop 0\n\tglobal_load_lds_dwordx4 %1, %2\n\ts_mov_b32 m0, %0"
               : "=&s"(keep) : "v"(voff), "s"(sbase), "s"(lds_dst) : "memory");
}
#define GLOAD16(dst, ptr, off) \
  asm volatile("global_load_dwordx4 %0, %1, off offset:%2" : "=v"(dst) : "v"(ptr), "i"(off) : "memory")
#define GLOAD16S(dst, voff, sbase, off) \
  asm volatile("global_load_dwordx4 %0, %1, %2 offset:%3" : "=v"(dst) : "v"(voff), "s"(sbase), "i"(off) : "memory")
#define WAITVM(n) asm volatile("s_waitcnt vmcnt(" #n ")" ::: "memory")
#define BARRIER() asm volatile("s_barrier" ::: "memory")
#define STAMP(i)
#define RSTAMP(i)

#define K1_ST(ptr, v) (*(ptr) = (v))
#define K1_MAP 0
constexpr int K1_SLOT = 53248;
constexpr int K1_BOFF = 32768;
constexpr int K1_HP = 176;
constexpr int K1_PQO = 49152;
constexpr int K1_KVO = 73728;
constexpr int K1_CSO = 110592;
constexpr int K1_STO = 114688;
constexpr int K1_CFO = 118784;

__global__ __launch_bounds__(1024, 4) void k1_kernel(const char* __restrict__ x, const char* __restrict__ wqkv,
                                                    const char* __restrict__ w1, const char* __restrict__ w3,
                                                    char* __restrict__ ws, const char* __restrict__ w2,
                                                    const char* __restrict__ wo) {
  __shared__ __attribute__((aligned(16))) char smem[3 * K1_SLOT];
  const int tid = threadIdx.x, lane = tid & 63;
  const int w = __builtin_amdgcn_readfirstlane(tid >> 6);
  const int b = blockIdx.x, xcd = b & 7, idx = b >> 3;
  const int mt = 4 * (xcd & 1) + (idx & 3), gidx = 8 * (xcd >> 1) + (idx >> 2);
  const int jj = gidx >> 1, typeB = gidx & 1;
  const int fc0 = 112 * jj + (typeB ? 80 : 0);
  const int m = lane & 15, g = lane >> 4, q = (lane >> 2) & 3, p = lane & 3;
  const int r32 = lane & 31, hh = lane >> 5;
  const int hw = w & 3;
  const unsigned lds0 = (unsigned)(uintptr_t)(LAS char*)smem;

  if (b == 0 && tid == 0) *(unsigned*)(ws + WS_CNT) = 0u;
#define DIAGOFF 0
  STAMP(0); RSTAMP(20);

  if (w >= 12) {
    const unsigned lrow = lds0 + (64 * hw + m) * 128 + ((g ^ ((m >> 1) & 7)) * 16);
    const bf16x8 ones8 = {0x3f80, 0x3f80, 0x3f80, 0x3f80, 0x3f80, 0x3f80, 0x3f80, 0x3f80};
    const unsigned lbw = lds0 + K1_BOFF + (32 * (hw & 1) + 4 * g + q) * 320 + (hw >> 1) * 160 + 8 * p;
    f32x4 cs[5];
#pragma unroll
    for (int t = 0; t < 5; ++t) cs[t] = f32x4{0.f, 0.f, 0.f, 0.f};
    f32x4 g2[4], g1[4];
#pragma unroll
    for (int i = 0; i < 4; ++i) { g2[i] = f32x4{0.f, 0.f, 0.f, 0.f}; g1[i] = f32x4{0.f, 0.f, 0.f, 0.f}; }
#pragma unroll
    for (int st = 0; st < 7; ++st) {
      BARRIER();
      unsigned lr2 = lrow + (st % 3) * K1_SLOT, lb2 = lbw + (st % 3) * K1_SLOT;
      asm volatile("" : "+v"(lr2), "+v"(lb2));
#pragma unroll
      for (int ti = 0; ti < 5; ++ti)
        cs[ti] = mfma16(ones8, cat8(tr_read(lb2 + 32 * ti), tr_read(lb2 + 32 * ti + 16 * 320)), cs[ti]);
#pragma unroll
      for (int ks = 0; ks < 2; ++ks)
#pragma unroll
        for (int i = 0; i < 4; ++i) {
          const bf16x8 af = *(LAS bf16x8*)(uintptr_t)((lr2 ^ (64u * ks)) + i * 2048);
          g2[i] = mfma16(af, af, g2[i]);
          g1[i] = mfma16(ones8, af, g1[i]);
        }
    }
    if (g == 0) {
#pragma unroll
      for (int ti = 0; ti < 5; ++ti)
        *(float*)(smem + K1_CSO + (ti * 32 + 16 * (hw >> 1) + m) * 8 + (hw & 1) * 4) = cs[ti][0];
    }
    if (g == (m >> 2)) {
#pragma unroll
      for (int i = 0; i < 4; ++i) {
        const int r = m & 3;
        const float s2 = r == 0 ? g2[i][0] : (r == 1 ? g2[i][1] : (r == 2 ? g2[i][2] : g2[i][3]));
        const float mean = g1[i][0] * (1.f / 448.f);
        const float var = fmaxf(s2 * (1.f / 448.f) - mean * mean, 0.f);
        *(f32x2*)(smem + K1_STO + (64 * hw + 16 * i + m) * 8) =
            f32x2{-rbf(mean), __builtin_amdgcn_rcpf(rbf(__builtin_amdgcn_sqrtf(rbf(rbf(var) + 1e-5f))))};
      }
    }
    __syncthreads();
    if (tid - 768 < 160) {
      const f32x2 c2 = *(const f32x2*)(smem + K1_CSO + (tid - 768) * 8);
      *(float*)(smem + K1_CFO + (tid - 768) * 4) = c2[0] + c2[1];
    }
    __syncthreads();
  } else if (w >= 8) {
    const char* xb = x + (size_t)mt * 256 * 896;
    unsigned aoff[2];
#pragma unroll
    for (int par = 0; par < 2; ++par)
      aoff[par] = (unsigned)(64 * hw + (lane >> 3)) * 896 + (((lane & 7) ^ ((4 * par + (lane >> 4)) & 7)) * 16);
#pragma unroll
    for (int u = 0; u < 8; ++u) dma16s(aoff[u & 1], xb + 8 * 896 * u, lds0 + (8 * hw + u) * 1024);
    const char* src[5];
    unsigned inc[5];
#pragma unroll
    for (int u = 0; u < 5; ++u) {
      const int o = 1024 * (5 * hw + u) + 16 * lane;
      const int r = o / 320, cb = o - r * 320;
      const int P = cb >> 5;
      const int Pm = P >= 5 ? P - 5 : P, hi = P >= 5 ? 1 : 0;
      const bool isq = typeB && Pm >= 2;
      const char* base = isq ? wqkv : (hi ? w3 : w1);
      const int col = isq ? (Pm == 2 ? 0 : (Pm == 3 ? 448 : 892)) + 28 * jj + 16 * hi : fc0 + 16 * Pm;
      src[u] = base + (size_t)r * (isq ? 2688 : 3584) + col * 2 + (cb & 16);
      inc[u] = isq ? 172032u : 229376u;
    }
    auto issueB = [&](int st) {
#pragma unroll
      for (int u = 0; u < 5; ++u) {
        dma16(src[u], lds0 + (st % 3) * K1_SLOT + K1_BOFF + (5 * hw + u) * 1024);
        src[u] += inc[u];
      }
    };
    auto issueA = [&](int st) {
#pragma unroll
      for (int u = 0; u < 8; ++u)
        dma16s(aoff[u & 1], xb + 8 * 896 * u + 128 * st, lds0 + (st % 3) * K1_SLOT + (8 * hw + u) * 1024);
    };
    issueB(0); issueA(1); issueB(1);
#pragma unroll
    for (int st = 0; st < 7; ++st) {
      if (st < 6) WAITVM(13); else WAITVM(0);
      BARRIER();
      if (st + 2 < 7) { issueA(st + 2); issueB(st + 2); }
    }
    {
      u32x4 d0, d1;
      const unsigned o1 = (unsigned)b * 6272u + (unsigned)(tid - 512) * 16u;
      const unsigned o2 = (unsigned)b * 1568u + (unsigned)(tid - 512) * 16u;
      GLOAD16S(d0, o1, w2, 0);
      if (tid - 512 < 136) GLOAD16S(d1, o1 + 4096u, w2, 0);
      if (tid - 512 < 98) GLOAD16S(d1, o2, wo, 0);
      WAITVM(0);
    }
    __syncthreads();
    __syncthreads();
  } else {
    const unsigned lb = lds0 + K1_BOFF + (8 * (g >> 1) + q) * 320 + (g & 1) * 160 + 8 * p;
    const unsigned la = lds0 + (w * 32 + r32) * 128 + ((hh ^ ((r32 >> 1) & 7)) * 16);
    f32x16 acc[5];
#pragma unroll
    for (int t = 0; t < 5; ++t)
#pragma unroll
      for (int e = 0; e < 16; ++e) acc[t][e] = 0.f;
#pragma unroll
    for (int st = 0; st < 7; ++st) {
      STAMP(2 + 2 * st);
      BARRIER();
      STAMP(3 + 2 * st);
      constexpr int PF = 4;
      unsigned lbs = lb + (st % 3) * K1_SLOT, las = la + (st % 3) * K1_SLOT;
      asm volatile("" : "+v"(lbs), "+v"(las));
      bf16x8 wf[PF + 1], xa[2];
      xa[0] = *(LAS bf16x8*)(uintptr_t)(las);
#pragma unroll
      for (int n = 0; n < PF; ++n) {
        const unsigned ad = lbs + (16 * (n / 5)) * 320 + 32 * (n % 5);
        wf[n % (PF + 1)] = cat8(tr_read(ad), tr_read(ad + 4 * 320));
      }
#pragma unroll
      for (int n = 0; n < 20; ++n) {
        if (n % 5 == 1 && n < 15) xa[(n / 5 + 1) & 1] = *(LAS bf16x8*)(uintptr_t)(las ^ (32u * (n / 5 + 1)));
        if (n + PF < 20) {
          const unsigned ad = lbs + (16 * ((n + PF) / 5)) * 320 + 32 * ((n + PF) % 5);
          wf[(n + PF) % (PF + 1)] = cat8(tr_read(ad), tr_read(ad + 4 * 320));
        }
        acc[n % 5] = __builtin_amdgcn_mfma_f32_32x32x16_bf16(wf[n % (PF + 1)], xa[(n / 5) & 1], acc[n % 5], 0, 0, 0);
        __builtin_amdgcn_sched_barrier(0);
      }
    }
    STAMP(16);
    __syncthreads();
    __syncthreads();

    const int rl = w * 32 + r32;
    const f32x2 st2 = *(const f32x2*)(smem + K1_STO + rl * 8);
    const float nm = st2[0], rr = st2[1];
    const float kx = rr * -1.44269504f, rr2 = rr * rr;
#pragma unroll
    for (int ti = 0; ti < 5; ++ti) {
      float t[16];
#pragma unroll
      for (int a4 = 0; a4 < 4; ++a4) {
        const f32x4 c4 = *(const f32x4*)(smem + K1_CFO + (ti * 32 + 8 * a4 + 4 * hh) * 4);
#pragma unroll
        for (int r = 0; r < 4; ++r) t[4 * a4 + r] = fmaf(nm, c4[r], acc[ti][4 * a4 + r]);
      }
      if (ti < 2 || !typeB) {
#pragma unroll
        for (int a2 = 0; a2 < 2; ++a2) {
          float hv[4];
#pragma unroll
          for (int r = 0; r < 4; ++r) {
            const float tg = t[4 * a2 + r], tu = t[8 + 4 * a2 + r];
            hv[r] = (tg * tu) * (rr2 * __builtin_amdgcn_rcpf(1.f + __builtin_amdgcn_exp2f(tg * kx)));
          }
          *(u32x2*)(smem + rl * K1_HP + 32 * ti + 16 * a2 + 8 * hh) = u32x2{pack_bf16(hv[0], hv[1]), pack_bf16(hv[2], hv[3])};
        }
      } else {
        const int sw = (rl >> 1) & 3;
#pragma unroll
        for (int a4 = 0; a4 < 4; ++a4) {
          float f[4];
#pragma unroll
          for (int r = 0; r < 4; ++r) {
            const float y = t[4 * a4 + r] * rr;
            if (ti < 4) f[r] = (a4 == 3 && hh) ? 0.f : phi_f(y);
            else        f[r] = (a4 == 0 && !hh) ? (r == 0 ? 1.f : 0.f) : y;
          }
          const u32x2 o = {pack_bf16(f[0], f[1]), pack_bf16(f[2], f[3])};
          if (ti == 2) *(u32x2*)(smem + K1_PQO + rl * 80 + 16 * a4 + 8 * hh) = o;
          else *(u32x2*)(smem + K1_KVO + rl * 128 + (((2 * (ti - 3) + (a4 >> 1)) ^ sw) * 32) + 16 * (a4 & 1) + 8 * hh) = o;
        }
      }
      __builtin_amdgcn_sched_barrier(0);
    }
    STAMP(17);
  }
  __syncthreads();

  char* hb = ws + WS_H + ((size_t)mt * 256 * F_ + fc0) * 2;
  if (!typeB) {
#pragma unroll
    for (int u = 0; u < 3; ++u) {
      const int c = tid + 1024 * u;
      if (u < 2 || c < 2560) {
        const int row = c / 10, ch = c - row * 10;
        K1_ST((u32x4*)(hb + (size_t)row * (F_ * 2) + ch * 16), *(const u32x4*)(smem + row * K1_HP + ch * 16));
      }
    }
  } else {
    if (w < 8) {
#pragma unroll
      for (int u = 0; u < 2; ++u) {
        const int c = tid + 512 * u, row = c >> 2, ch = c & 3;
        K1_ST((u32x4*)(hb + (size_t)row * (F_ * 2) + ch * 16), *(const u32x4*)(smem + row * K1_HP + ch * 16));
        K1_ST((u32x4*)(ws + WS_PQ + ((size_t)(mt * 256 + row) * 512 + 32 * jj) * 2 + ch * 16),
              *(const u32x4*)(smem + K1_PQO + row * 80 + ch * 16));
      }
    } else if (w >= 12) {
      const int dc = hw >> 1, ec = hw & 1;
      const int sig = (2 * g + (q >> 1)) & 3;
      const unsigned lk = lds0 + K1_KVO + (4 * g + q) * 128 + ((dc ^ sig) * 32) + 8 * p;
      const unsigned lv = lds0 + K1_KVO + (4 * g + q) * 128 + (((2 + ec) ^ sig) * 32) + 8 * p;
      f32x4 kv = f32x4{0.f, 0.f, 0.f, 0.f};
#pragma unroll
      for (int ks = 0; ks < 8; ++ks) {
        bf16x8 af = cat8(tr_read(lk + ks * 4096), tr_read(lk + ks * 4096 + 2048));
        bf16x8 bfr = cat8(tr_read(lv + ks * 4096), tr_read(lv + ks * 4096 + 2048));
        kv = mfma16(af, bfr, kv);
      }
      *(f32x4*)(ws + WS_P + ((size_t)(jj * 8 + mt) * 1024 + (16 * ec + m) * 32 + 16 * dc + 4 * g) * 4) = kv;
    }
  }
  STAMP(18); RSTAMP(21);
#undef DIAGOFF
}

constexpr int K2_SLOT = 8192;
constexpr int K2_RSTR = 272;
constexpr int K2_PART = 64 * K2_RSTR;

__global__ __launch_bounds__(512, 2) void k2_kernel(const char* __restrict__ x, const char* __restrict__ wo,
                                                   const char* __restrict__ w2, char* ws, char* __restrict__ out) {
  __shared__ __attribute__((aligned(16))) char smem[20 * K2_SLOT];
  const int tid = threadIdx.x, lane = tid & 63;
  const int w8 = __builtin_amdgcn_readfirstlane(tid >> 6);
  const int w = w8 & 3;
  const int b = blockIdx.x, xcd = b & 7, idx = b >> 3;
  const int mtile = xcd * 4 + (idx & 3), nt = idx >> 2;
  const int R0 = mtile * 64, C0 = nt * 64;
  const int m = lane & 15, g = lane >> 4, q = (lane >> 2) & 3, p = lane & 3;
  const unsigned lds0 = (unsigned)(uintptr_t)(LAS char*)smem;
  const unsigned ring = lds0 + w * 5 * K2_SLOT;
#define DIAGOFF (8u << 20)
  STAMP(0); RSTAMP(20);

  u32x4 xv;
  const unsigned go = (unsigned)(((R0 + (tid >> 3)) * D_ + C0 + (tid & 7) * 8) * 2);
  GLOAD16S(xv, go, x, 0);

  f32x4 acc[4][4], acs[4][4];
  if (w8 >= 4) {
    const int lr = lane >> 3, cbk = lane & 7;
    unsigned aoff[2], qoff[2];
#pragma unroll
    for (int par = 0; par < 2; ++par) {
      const int ch = cbk ^ ((4 * par + (lr >> 1)) & 7);
      aoff[par] = (unsigned)(WS_H + ((size_t)(R0 + lr) * F_ + 448 * w + 8 * ch) * 2);
      qoff[par] = (unsigned)(WS_PQ + ((size_t)(R0 + lr) * 512 + 128 * w + 8 * ch) * 2);
    }
    const int tnd = (cbk >> 1) ^ ((lane >> 4) & 3);
    const unsigned bcol = (unsigned)(C0 + 16 * tnd + 8 * (cbk & 1)) * 2;
    const unsigned boff = (unsigned)(8 * (lr >> 2) + (lr & 3)) * 896 + bcol;
    auto issue = [&](int hu) {
      const unsigned sb = ring + (hu % 5) * K2_SLOT;
      const int un = hu >> 1;
      const bool att = un >= 7;
      const int c = un;
      if (!(hu & 1)) {
        if (!att) {
#pragma unroll
          for (int u = 0; u < 8; ++u) dma16s(aoff[u & 1], ws + (size_t)u * 8 * F_ * 2 + 128 * c, sb + u * 1024);
        } else {
#pragma unroll
          for (int u = 0; u < 8; ++u) dma16s(qoff[u & 1], ws + (size_t)u * 8 * 1024 + 128 * (un - 7), sb + u * 1024);
        }
      } else {
        if (!att) {
          const char* wb = w2 + (size_t)(448 * w + 64 * c) * 896;
#pragma unroll
          for (int u = 0; u < 8; ++u) {
            const int kb = 32 * (u >> 2) + ((u & 1) ? 16 : 0) + ((u & 2) ? 4 : 0);
            dma16s(boff, wb + kb * 896, sb + u * 1024);
          }
        } else {
#pragma unroll
          for (int u = 0; u < 8; ++u) {
            const int e = min(8 * (u & 3) + lr, 27);
            const unsigned vo = (unsigned)(28 * (4 * w + 2 * (un - 7) + (u >> 2)) + e) * 896 + bcol;
            dma16s(vo, wo, sb + u * 1024);
          }
        }
      }
    };
    issue(0); issue(1); issue(2); issue(3); issue(4);
#pragma unroll
    for (int un = 0; un < 9; ++un) {
      if (un == 0) WAITVM(24); else if (un < 8) WAITVM(8); else WAITVM(0);
      BARRIER();
      if (un >= 1) {
        if (2 * un + 3 < 18) issue(2 * un + 3);
        if (2 * un + 4 < 18) issue(2 * un + 4);
      }
    }
  } else {
    if (b < 64 && w == 3) {
      const int head = b >> 2, pos = 8 * (b & 3) + (lane >> 3), d4 = (lane & 7) * 4;
      const float* P = (const float*)(ws + WS_P) + (size_t)head * 8 * 1024 + pos * 32 + d4;
      f32x4 v[8];
#pragma unroll
      for (int t = 0; t < 8; ++t) v[t] = *(const f32x4*)(P + t * 1024);
      f32x4 sacc = v[0];
#pragma unroll
      for (int t = 1; t < 8; ++t) sacc += v[t];
      const int e = pos >= 4 ? pos - 4 : pos + 28;
      unsigned long long o = (unsigned long long)pack_bf16(sacc[0], sacc[1]) |
                             ((unsigned long long)pack_bf16(sacc[2], sacc[3]) << 32);
      __hip_atomic_store((unsigned long long*)(ws + WS_KVT + ((size_t)(head * 32 + e) * 32 + d4) * 2), o,
                         __ATOMIC_RELAXED, __HIP_MEMORY_SCOPE_AGENT);
      WAITVM(0);
      if (lane == 0) __hip_atomic_fetch_add((unsigned*)(ws + WS_CNT), 1u, __ATOMIC_RELAXED, __HIP_MEMORY_SCOPE_AGENT);
    }
    STAMP(1);
    const int sig = (2 * g + (q >> 1)) & 3;
    unsigned lb[4], la[2];
#pragma unroll
    for (int xx = 0; xx < 4; ++xx) lb[xx] = ring + (4 * g + q) * 128 + ((xx ^ sig) * 32) + 8 * p;
#pragma unroll
    for (int ks = 0; ks < 2; ++ks) la[ks] = ring + m * 128 + (((4 * ks + g) ^ ((m >> 1) & 7)) * 16);
#pragma unroll
    for (int i = 0; i < 4; ++i)
#pragma unroll
      for (int t = 0; t < 4; ++t) { acc[i][t] = f32x4{0.f, 0.f, 0.f, 0.f}; acs[i][t] = f32x4{0.f, 0.f, 0.f, 0.f}; }
    u32x4 kvf[4][3];
    unsigned cnt0;
#pragma unroll
    for (int un = 0; un < 9; ++un) {
      const int sa = ((2 * un) % 5) * K2_SLOT, sbb = ((2 * un + 1) % 5) * K2_SLOT;
      BARRIER();
      if (un == 5) {
        asm volatile("global_load_dword %0, %1, %2 sc1" : "=v"(cnt0) : "v"((unsigned)WS_CNT), "s"(ws) : "memory");
      }
      if (un == 6) {
        asm volatile("s_waitcnt vmcnt(0)" : "+v"(cnt0) :: "memory");
        if (__builtin_amdgcn_readfirstlane(cnt0) < 64u) {
          const unsigned* cnt = (const unsigned*)(ws + WS_CNT);
          while (__hip_atomic_load(cnt, __ATOMIC_RELAXED, __HIP_MEMORY_SCOPE_AGENT) < 64u) __builtin_amdgcn_s_sleep(8);
        }
        const unsigned ko = (unsigned)(WS_KVT + ((size_t)(4 * w * 32 + m) * 32 + 8 * g) * 2);
        const unsigned zo = (unsigned)(WS_KVT + ((size_t)(4 * w * 32 + 28) * 32 + 8 * g) * 2);
#pragma unroll
        for (int h4 = 0; h4 < 4; ++h4) {
#pragma unroll
          for (int ec = 0; ec < 2; ++ec)
            asm volatile("global_load_dwordx4 %0, %1, %2 offset:%3 sc1" : "=v"(kvf[h4][ec]) : "v"(ko + (h4 >> 1) * 4096),
                         "s"(ws), "i"(((h4 & 1) * 32 + ec * 16) * 64) : "memory");
          asm volatile("global_load_dwordx4 %0, %1, %2 offset:%3 sc1" : "=v"(kvf[h4][2]) : "v"(zo + (h4 >> 1) * 4096),
                       "s"(ws), "i"((h4 & 1) * 2048) : "memory");
        }
      }
      if (un == 7) {
        asm volatile("s_waitcnt vmcnt(0)" : "+v"(kvf[0][0]), "+v"(kvf[0][1]), "+v"(kvf[1][0]), "+v"(kvf[1][1]), "+v"(kvf[2][0]),
                     "+v"(kvf[2][1]), "+v"(kvf[3][0]), "+v"(kvf[3][1]), "+v"(kvf[0][2]), "+v"(kvf[1][2]), "+v"(kvf[2][2]),
                     "+v"(kvf[3][2]) :: "memory");
        __builtin_amdgcn_sched_barrier(0);
      }
      if (un < 7) {
#pragma unroll
        for (int ks = 0; ks < 2; ++ks) {
          bf16x8 wf[4], af[4];
#pragma unroll
          for (int t = 0; t < 4; ++t) wf[t] = cat8(tr_read(lb[t] + sbb + ks * 4096), tr_read(lb[t] + sbb + ks * 4096 + 2048));
#pragma unroll
          for (int i = 0; i < 4; ++i) af[i] = *(LAS bf16x8*)(uintptr_t)(la[ks] + sa + i * 2048);
#pragma unroll
          for (int t = 0; t < 4; ++t)
#pragma unroll
            for (int i = 0; i < 4; ++i) acc[i][t] = mfma16(wf[t], af[i], acc[i][t]);
        }
      } else {
        f32x4 n0[2][4], n1[2][4], dz[2][4];
#pragma unroll
        for (int ks = 0; ks < 2; ++ks) {
          const int h4 = 2 * (un - 7) + ks;
#pragma unroll
          for (int i = 0; i < 4; ++i) {
            const bf16x8 af = *(LAS bf16x8*)(uintptr_t)(la[ks] + sa + i * 2048);
            dz[ks][i] = mfma16(__builtin_bit_cast(bf16x8, kvf[h4][2]), af, f32x4{0.f, 0.f, 0.f, 0.f});
            n1[ks][i] = mfma16(__builtin_bit_cast(bf16x8, kvf[h4][1]), af, f32x4{0.f, 0.f, 0.f, 0.f});
            n0[ks][i] = mfma16(__builtin_bit_cast(bf16x8, kvf[h4][0]), af, f32x4{0.f, 0.f, 0.f, 0.f});
          }
        }
#pragma unroll
        for (int ks = 0; ks < 2; ++ks) {
          bf16x8 wf[4];
#pragma unroll
          for (int t = 0; t < 4; ++t) wf[t] = cat8(tr_read(lb[t] + sbb + ks * 4096), tr_read(lb[t] + sbb + ks * 4096 + 2048));
#pragma unroll
          for (int i = 0; i < 4; ++i) {
            const float rden = __builtin_amdgcn_rcpf(dz[ks][i][0] + 1e-6f);
            const float rd1 = g == 3 ? 0.f : rden;
            u32x4 ob = {pack_bf16(n0[ks][i][0] * rden, n0[ks][i][1] * rden), pack_bf16(n0[ks][i][2] * rden, n0[ks][i][3] * rden),
                        pack_bf16(n1[ks][i][0] * rd1, n1[ks][i][1] * rd1), pack_bf16(n1[ks][i][2] * rd1, n1[ks][i][3] * rd1)};
            const bf16x8 of = __builtin_bit_cast(bf16x8, ob);
#pragma unroll
            for (int t = 0; t < 4; ++t) acs[i][t] = mfma16(wf[t], of, acs[i][t]);
          }
        }
      }
      __builtin_amdgcn_sched_barrier(0);
      STAMP(un + 2);
    }
  }

  STAMP(18);
  __syncthreads();
  if (w8 < 4) {
#pragma unroll
    for (int i = 0; i < 4; ++i)
#pragma unroll
      for (int t = 0; t < 4; ++t) {
        char* pb = smem + (16 * i + m) * K2_RSTR + (16 * t + 4 * g) * 4;
        *(f32x4*)(pb + (2 * w) * K2_PART) = acc[i][t];
        *(f32x4*)(pb + (2 * w + 1) * K2_PART) = acs[i][t];
      }
  }
  WAITVM(0);
  __syncthreads();
  asm volatile("" : "+v"(xv));
  {
    const int row = tid >> 3, n0 = (tid & 7) * 8;
    unsigned res[4];
#pragma unroll
    for (int v4 = 0; v4 < 2; ++v4) {
      const char* pb = smem + row * K2_RSTR + (n0 + 4 * v4) * 4;
      f32x4 so = *(const f32x4*)(pb), ss = *(const f32x4*)(pb + K2_PART);
#pragma unroll
      for (int u = 1; u < 4; ++u) {
        so += *(const f32x4*)(pb + (2 * u) * K2_PART);
        ss += *(const f32x4*)(pb + (2 * u + 1) * K2_PART);
      }
      float y[4];
#pragma unroll
      for (int r = 0; r < 4; ++r) {
        const unsigned xu = xv[2 * v4 + (r >> 1)];
        const float xf = (r & 1) ? bf_hi(xu) : bf_lo(xu);
        y[r] = xf + ss[r] * silu_f(so[r]);
      }
      res[2 * v4] = pack_bf16(y[0], y[1]);
      res[2 * v4 + 1] = pack_bf16(y[2], y[3]);
    }
    *(u32x4*)(out + go) = u32x4{res[0], res[1], res[2], res[3]};
  }
  STAMP(19); RSTAMP(21);
#undef DIAGOFF
}

extern "C" void kernel_launch(void* const* d_in, const int* in_sizes, int n_in,
                              void* d_out, int out_size, void* d_ws, size_t ws_size,
                              hipStream_t stream) {
  const char* x    = (const char*)d_in[0];
  const char* wqkv = (const char*)d_in[1];
  const char* wo   = (const char*)d_in[2];
  const char* w1   = (const char*)d_in[4];
  const char* w3   = (const char*)d_in[5];
  const char* w2   = (const char*)d_in[6];
  k1_kernel<<<256, 1024, 0, stream>>>(x, wqkv, w1, w3, (char*)d_ws, w2, wo);
  k2_kernel<<<224, 512, 0, stream>>>(x, wo, w2, (char*)d_ws, (char*)d_out);
}
```

```cpp
#include <hip/hip_runtime.h>
#include <hip/hip_bf16.h>
#include <stdint.h>

typedef short    bf16x8 __attribute__((ext_vector_type(8)));
typedef short    bf16x4 __attribute__((ext_vector_type(4)));
typedef float    f32x4  __attribute__((ext_vector_type(4)));
typedef float    f32x2  __attribute__((ext_vector_type(2)));
typedef float    f32x16 __attribute__((ext_vector_type(16)));
typedef unsigned u32x4  __attribute__((ext_vector_type(4)));
typedef unsigned u32x2  __attribute__((ext_vector_type(2)));
typedef __bf16   bf2_t  __attribute__((ext_vector_type(2)));
#define LAS __attribute__((address_space(3)))

constexpr int S_ = 2048, D_ = 448, F_ = 1792;
constexpr size_t WS_H   = 0;
constexpr size_t WS_PQ  = WS_H + (size_t)S_ * F_ * 2;
constexpr size_t WS_P   = WS_PQ + (size_t)S_ * 512 * 2;
constexpr size_t WS_KVT = WS_P + (size_t)16 * 16 * 1024 * 4;
constexpr size_t WS_CNT = WS_KVT + (size_t)16 * 1024 * 2;

__device__ __forceinline__ unsigned pack_bf16(float lo, float hi) {
  f32x2 v = {lo, hi};
  bf2_t r = __builtin_convertvector(v, bf2_t);
  return __builtin_bit_cast(unsigned, r);
}
__device__ __forceinline__ float bf_lo(unsigned u) { return __uint_as_float(u << 16); }
__device__ __forceinline__ float bf_hi(unsigned u) { return __uint_as_float(u & 0xffff0000u); }
__device__ __forceinline__ float rbf(float f) { return bf_lo(pack_bf16(f, 0.f) & 0xffffu); }
__device__ __forceinline__ float silu_f(float v) { return v * __builtin_amdgcn_rcpf(1.f + __expf(-v)); }
__device__ __forceinline__ float phi_f(float t) { return (t > 0.f ? t + 1.f : __expf(t)) + 1e-6f; }
__device__ __forceinline__ f32x4 mfma16(bf16x8 a, bf16x8 b, f32x4 c) {
  return __builtin_amdgcn_mfma_f32_16x16x32_bf16(a, b, c, 0, 0, 0);
}
__device__ __forceinline__ bf16x4 tr_read(unsigned lds_addr) {
  return __builtin_amdgcn_ds_read_tr16_b64_v4i16((LAS bf16x4*)(uintptr_t)lds_addr);
}
__device__ __forceinline__ bf16x8 cat8(bf16x4 lo, bf16x4 hi) {
  return __builtin_shufflevector(lo, hi, 0, 1, 2, 3, 4, 5, 6, 7);
}
__device__ __forceinline__ void dma16(const void* gsrc, unsigned lds_dst) {
  unsigned keep;
  asm volatile("s_mov_b32 %0, m0\n\ts_mov_b32 m0, %2\n\ts_nop 0\n\tglobal_load_lds_dwordx4 %1, off\n\ts_mov_b32 m0, %0"
               : "=&s"(keep) : "v"(gsrc), "s"(lds_dst) : "memory");
}
__device__ __forceinline__ void dma16s(unsigned voff, const void* sbase, unsigned lds_dst) {
  unsigned keep;
  asm volatile("s_mov_b32 %0, m0\n\ts_mov_b32 m0, %3\n\ts_nop 0\n\tglobal_load_lds_dwordx4 %1, %2\n\ts_mov_b32 m0, %0"
               : "=&s"(keep) : "v"(voff), "s"(sbase), "s"(lds_dst) : "memory");
}
#define GLOAD16(dst, ptr, off) \
  asm volatile("global_load_dwordx4 %0, %1, off offset:%2" : "=v"(dst) : "v"(ptr), "i"(off) : "memory")
#define GLOAD16S(dst, voff, sbase, off) \
  asm volatile("global_load_dwordx4 %0, %1, %2 offset:%3" : "=v"(dst) : "v"(voff), "s"(sbase), "i"(off) : "memory")
#define WAITVM(n) asm volatile("s_waitcnt vmcnt(" #n ")" ::: "memory")
#define BARRIER() asm volatile("s_barrier" ::: "memory")

#define K1_ST(ptr, v) (*(ptr) = (v))
constexpr int K1_SLOT = 53248;
constexpr int K1_BOFF = 32768;
constexpr int K1_HP = 176;
constexpr int K1_PQO = 49152;
constexpr int K1_KVO = 73728;
constexpr int K1_CSO = 110592;
constexpr int K1_STO = 114688;
constexpr int K1_CFO = 118784;

__global__ __launch_bounds__(1024, 4) void k1_kernel(const char* __restrict__ x, const char* __restrict__ wqkv,
                                                    const char* __restrict__ w1, const char* __restrict__ w3,
                                                    char* __restrict__ ws, const char* __restrict__ w2,
                                                    const char* __restrict__ wo) {
  __shared__ __attribute__((aligned(16))) char smem[3 * K1_SLOT];
  const int tid = threadIdx.x, lane = tid & 63;
  const int w = __builtin_amdgcn_readfirstlane(tid >> 6);
  const int b = blockIdx.x, xcd = b & 7, idx = b >> 3;
  const int mt = 4 * (xcd & 1) + (idx & 3), gidx = 8 * (xcd >> 1) + (idx >> 2);
  const int jj = gidx >> 1, typeB = gidx & 1;
  const int fc0 = 112 * jj + (typeB ? 80 : 0);
  const int m = lane & 15, g = lane >> 4, q = (lane >> 2) & 3, p = lane & 3;
  const int r32 = lane & 31, hh = lane >> 5;
  const int hw = w & 3;
  const unsigned lds0 = (unsigned)(uintptr_t)(LAS char*)smem;

  if (b == 0 && tid == 0) *(unsigned*)(ws + WS_CNT) = 0u;

  if (w >= 12) {
    const unsigned lrow = lds0 + (64 * hw + m) * 128 + ((g ^ ((m >> 1) & 7)) * 16);
    const bf16x8 ones8 = {0x3f80, 0x3f80, 0x3f80, 0x3f80, 0x3f80, 0x3f80, 0x3f80, 0x3f80};
    const unsigned lbw = lds0 + K1_BOFF + (32 * (hw & 1) + 4 * g + q) * 320 + (hw >> 1) * 160 + 8 * p;
    f32x4 cs[5];
#pragma unroll
    for (int t = 0; t < 5; ++t) cs[t] = f32x4{0.f, 0.f, 0.f, 0.f};
    f32x4 g2[4], g1[4];
#pragma unroll
    for (int i = 0; i < 4; ++i) { g2[i] = f32x4{0.f, 0.f, 0.f, 0.f}; g1[i] = f32x4{0.f, 0.f, 0.f, 0.f}; }
#pragma unroll
    for (int st = 0; st < 7; ++st) {
      BARRIER();
      unsigned lr2 = lrow + (st % 3) * K1_SLOT, lb2 = lbw + (st % 3) * K1_SLOT;
      asm volatile("" : "+v"(lr2), "+v"(lb2));
#pragma unroll
      for (int ti = 0; ti < 5; ++ti)
        cs[ti] = mfma16(ones8, cat8(tr_read(lb2 + 32 * ti), tr_read(lb2 + 32 * ti + 16 * 320)), cs[ti]);
#pragma unroll
      for (int ks = 0; ks < 2; ++ks)
#pragma unroll
        for (int i = 0; i < 4; ++i) {
          const bf16x8 af = *(LAS bf16x8*)(uintptr_t)((lr2 ^ (64u * ks)) + i * 2048);
          g2[i] = mfma16(af, af, g2[i]);
          g1[i] = mfma16(ones8, af, g1[i]);
        }
    }
    if (g == 0) {
#pragma unroll
      for (int ti = 0; ti < 5; ++ti)
        *(float*)(smem + K1_CSO + (ti * 32 + 16 * (hw >> 1) + m) * 8 + (hw & 1) * 4) = cs[ti][0];
    }
    if (g == (m >> 2)) {
#pragma unroll
      for (int i = 0; i < 4; ++i) {
        const int r = m & 3;
        const float s2 = r == 0 ? g2[i][0] : (r == 1 ? g2[i][1] : (r == 2 ? g2[i][2] : g2[i][3]));
        const float mean = g1[i][0] * (1.f / 448.f);
        const float var = fmaxf(s2 * (1.f / 448.f) - mean * mean, 0.f);
        *(f32x2*)(smem + K1_STO + (64 * hw + 16 * i + m) * 8) =
            f32x2{-rbf(mean), __builtin_amdgcn_rcpf(rbf(__builtin_amdgcn_sqrtf(rbf(rbf(var) + 1e-5f))))};
      }
    }
    __syncthreads();
    if (tid - 768 < 160) {
      const f32x2 c2 = *(const f32x2*)(smem + K1_CSO + (tid - 768) * 8);
      *(float*)(smem + K1_CFO + (tid - 768) * 4) = c2[0] + c2[1];
    }
    __syncthreads();
  } else if (w >= 8) {
    const char* xb = x + (size_t)mt * 256 * 896;
    unsigned aoff[2];
#pragma unroll
    for (int par = 0; par < 2; ++par)
      aoff[par] = (unsigned)(64 * hw + (lane >> 3)) * 896 + (((lane & 7) ^ ((4 * par + (lane >> 4)) & 7)) * 16);
#pragma unroll
    for (int u = 0; u < 8; ++u) dma16s(aoff[u & 1], xb + 8 * 896 * u, lds0 + (8 * hw + u) * 1024);
    const char* src[5];
    unsigned inc[5];
#pragma unroll
    for (int u = 0; u < 5; ++u) {
      const int o = 1024 * (5 * hw + u) + 16 * lane;
      const int r = o / 320, cb = o - r * 320;
      const int P = cb >> 5;
      const int Pm = P >= 5 ? P - 5 : P, hi = P >= 5 ? 1 : 0;
      const bool isq = typeB && Pm >= 2;
      const char* base = isq ? wqkv : (hi ? w3 : w1);
      const int col = isq ? (Pm == 2 ? 0 : (Pm == 3 ? 448 : 892)) + 28 * jj + 16 * hi : fc0 + 16 * Pm;
      src[u] = base + (size_t)r * (isq ? 2688 : 3584) + col * 2 + (cb & 16);
      inc[u] = isq ? 172032u : 229376u;
    }
    auto issueB = [&](int st) {
#pragma unroll
      for (int u = 0; u < 5; ++u) {
        dma16(src[u], lds0 + (st % 3) * K1_SLOT + K1_BOFF + (5 * hw + u) * 1024);
        src[u] += inc[u];
      }
    };
    auto issueA = [&](int st) {
#pragma unroll
      for (int u = 0; u < 8; ++u)
        dma16s(aoff[u & 1], xb + 8 * 896 * u + 128 * st, lds0 + (st % 3) * K1_SLOT + (8 * hw + u) * 1024);
    };
    issueB(0); issueA(1); issueB(1);
#pragma unroll
    for (int st = 0; st < 7; ++st) {
      if (st < 6) WAITVM(13); else WAITVM(0);
      BARRIER();
      if (st + 2 < 7) { issueA(st + 2); issueB(st + 2); }
    }
    {
      u32x4 d0, d1;
      const unsigned o1 = (unsigned)b * 6272u + (unsigned)(tid - 512) * 16u;
      const unsigned o2 = (unsigned)b * 1568u + (unsigned)(tid - 512) * 16u;
      GLOAD16S(d0, o1, w2, 0);
      if (tid - 512 < 136) GLOAD16S(d1, o1 + 4096u, w2, 0);
      if (tid - 512 < 98) GLOAD16S(d1, o2, wo, 0);
      WAITVM(0);
    }
    __syncthreads();
    __syncthreads();
  } else {
    const unsigned lb = lds0 + K1_BOFF + (8 * (g >> 1) + q) * 320 + (g & 1) * 160 + 8 * p;
    const unsigned la = lds0 + (w * 32 + r32) * 128 + ((hh ^ ((r32 >> 1) & 7)) * 16);
    f32x16 acc[5];
#pragma unroll
    for (int t = 0; t < 5; ++t)
#pragma unroll
      for (int e = 0; e < 16; ++e) acc[t][e] = 0.f;
#pragma unroll
    for (int st = 0; st < 7; ++st) {
      BARRIER();
      constexpr int PF = 4;
      unsigned lbs = lb + (st % 3) * K1_SLOT, las = la + (st % 3) * K1_SLOT;
      asm volatile("" : "+v"(lbs), "+v"(las));
      bf16x8 wf[PF + 1], xa[2];
      xa[0] = *(LAS bf16x8*)(uintptr_t)(las);
#pragma unroll
      for (int n = 0; n < PF; ++n) {
        const unsigned ad = lbs + (16 * (n / 5)) * 320 + 32 * (n % 5);
        wf[n % (PF + 1)] = cat8(tr_read(ad), tr_read(ad + 4 * 320));
      }
#pragma unroll
      for (int n = 0; n < 20; ++n) {
        if (n % 5 == 1 && n < 15) xa[(n / 5 + 1) & 1] = *(LAS bf16x8*)(uintptr_t)(las ^ (32u * (n / 5 + 1)));
        if (n + PF < 20) {
          const unsigned ad = lbs + (16 * ((n + PF) / 5)) * 320 + 32 * ((n + PF) % 5);
          wf[(n + PF) % (PF + 1)] = cat8(tr_read(ad), tr_read(ad + 4 * 320));
        }
        acc[n % 5] = __builtin_amdgcn_mfma_f32_32x32x16_bf16(wf[n % (PF + 1)], xa[(n / 5) & 1], acc[n % 5], 0, 0, 0);
        __builtin_amdgcn_sched_barrier(0);
      }
    }
    __syncthreads();
    __syncthreads();

    const int rl = w * 32 + r32;
    const f32x2 st2 = *(const f32x2*)(smem + K1_STO + rl * 8);
    const float nm = st2[0], rr = st2[1];
    const float kx = rr * -1.44269504f, rr2 = rr * rr;
#pragma unroll
    for (int ti = 0; ti < 5; ++ti) {
      float t[16];
#pragma unroll
      for (int a4 = 0; a4 < 4; ++a4) {
        const f32x4 c4 = *(const f32x4*)(smem + K1_CFO + (ti * 32 + 8 * a4 + 4 * hh) * 4);
#pragma unroll
        for (int r = 0; r < 4; ++r) t[4 * a4 + r] = fmaf(nm, c4[r], acc[ti][4 * a4 + r]);
      }
      if (ti < 2 || !typeB) {
#pragma unroll
        for (int a2 = 0; a2 < 2; ++a2) {
          float hv[4];
#pragma unroll
          for (int r = 0; r < 4; ++r) {
            const float tg = t[4 * a2 + r], tu = t[8 + 4 * a2 + r];
            hv[r] = (tg * tu) * (rr2 * __builtin_amdgcn_rcpf(1.f + __builtin_amdgcn_exp2f(tg * kx)));
          }
          *(u32x2*)(smem + rl * K1_HP + 32 * ti + 16 * a2 + 8 * hh) = u32x2{pack_bf16(hv[0], hv[1]), pack_bf16(hv[2], hv[3])};
        }
      } else {
        const int sw = (rl >> 1) & 3;
#pragma unroll
        for (int a4 = 0; a4 < 4; ++a4) {
          float f[4];
#pragma unroll
          for (int r = 0; r < 4; ++r) {
            const float y = t[4 * a4 + r] * rr;
            if (ti < 4) f[r] = (a4 == 3 && hh) ? 0.f : phi_f(y);
            else        f[r] = (a4 == 0 && !hh) ? (r == 0 ? 1.f : 0.f) : y;
          }
          const u32x2 o = {pack_bf16(f[0], f[1]), pack_bf16(f[2], f[3])};
          if (ti == 2) *(u32x2*)(smem + K1_PQO + rl * 80 + 16 * a4 + 8 * hh) = o;
          else *(u32x2*)(smem + K1_KVO + rl * 128 + (((2 * (ti - 3) + (a4 >> 1)) ^ sw) * 32) + 16 * (a4 & 1) + 8 * hh) = o;
        }
      }
      __builtin_amdgcn_sched_barrier(0);
    }
  }
  __syncthreads();

  char* hb = ws + WS_H + ((size_t)mt * 256 * F_ + fc0) * 2;
  if (!typeB) {
#pragma unroll
    for (int u = 0; u < 3; ++u) {
      const int c = tid + 1024 * u;
      if (u < 2 || c < 2560) {
        const int row = c / 10, ch = c - row * 10;
        K1_ST((u32x4*)(hb + (size_t)row * (F_ * 2) + ch * 16), *(const u32x4*)(smem + row * K1_HP + ch * 16));
      }
    }
  } else {
    if (w < 8) {
#pragma unroll
      for (int u = 0; u < 2; ++u) {
        const int c = tid + 512 * u, row = c >> 2, ch = c & 3;
        K1_ST((u32x4*)(hb + (size_t)row * (F_ * 2) + ch * 16), *(const u32x4*)(smem + row * K1_HP + ch * 16));
        K1_ST((u32x4*)(ws + WS_PQ + ((size_t)(mt * 256 + row) * 512 + 32 * jj) * 2 + ch * 16),
              *(const u32x4*)(smem + K1_PQO + row * 80 + ch * 16));
      }
    } else if (w >= 12) {
      const int dc = hw >> 1, ec = hw & 1;
      const int sig = (2 * g + (q >> 1)) & 3;
      const unsigned lk = lds0 + K1_KVO + (4 * g + q) * 128 + ((dc ^ sig) * 32) + 8 * p;
      const unsigned lv = lds0 + K1_KVO + (4 * g + q) * 128 + (((2 + ec) ^ sig) * 32) + 8 * p;
      f32x4 kv = f32x4{0.f, 0.f, 0.f, 0.f};
#pragma unroll
      for (int ks = 0; ks < 8; ++ks) {
        bf16x8 af = cat8(tr_read(lk + ks * 4096), tr_read(lk + ks * 4096 + 2048));
        bf16x8 bfr = cat8(tr_read(lv + ks * 4096), tr_read(lv + ks * 4096 + 2048));
        kv = mfma16(af, bfr, kv);
      }
      *(f32x4*)(ws + WS_P + ((size_t)(jj * 8 + mt) * 1024 + (16 * ec + m) * 32 + 16 * dc + 4 * g) * 4) = kv;
    }
  }
}

constexpr int K2_SLOT = 8192;
constexpr int K2_RSTR = 272;
constexpr int K2_PART = 64 * K2_RSTR;

__global__ __launch_bounds__(512, 2) void k2_kernel(const char* __restrict__ x, const char* __restrict__ wo,
                                                   const char* __restrict__ w2, char* ws, char* __restrict__ out) {
  __shared__ __attribute__((aligned(16))) char smem[20 * K2_SLOT];
  const int tid = threadIdx.x, lane = tid & 63;
  const int w8 = __builtin_amdgcn_readfirstlane(tid >> 6);
  const int w = w8 & 3;
  const int b = blockIdx.x, xcd = b & 7, idx = b >> 3;
  const int mtile = xcd * 4 + (idx & 3), nt = idx >> 2;
  const int R0 = mtile * 64, C0 = nt * 64;
  const int m = lane & 15, g = lane >> 4, q = (lane >> 2) & 3, p = lane & 3;
  const unsigned lds0 = (unsigned)(uintptr_t)(LAS char*)smem;
  const unsigned ring = lds0 + w * 5 * K2_SLOT;

  u32x4 xv;
  const unsigned go = (unsigned)(((R0 + (tid >> 3)) * D_ + C0 + (tid & 7) * 8) * 2);
  GLOAD16S(xv, go, x, 0);

  f32x4 acc[4][4], acs[4][4];
  if (w8 >= 4) {
    const int lr = lane >> 3, cbk = lane & 7;
    unsigned aoff[2], qoff[2];
#pragma unroll
    for (int par = 0; par < 2; ++par) {
      const int ch = cbk ^ ((4 * par + (lr >> 1)) & 7);
      aoff[par] = (unsigned)(WS_H + ((size_t)(R0 + lr) * F_ + 448 * w + 8 * ch) * 2);
      qoff[par] = (unsigned)(WS_PQ + ((size_t)(R0 + lr) * 512 + 128 * w + 8 * ch) * 2);
    }
    const int tnd = (cbk >> 1) ^ ((lane >> 4) & 3);
    const unsigned bcol = (unsigned)(C0 + 16 * tnd + 8 * (cbk & 1)) * 2;
    const unsigned boff = (unsigned)(8 * (lr >> 2) + (lr & 3)) * 896 + bcol;
    auto issue = [&](int hu) {
      const unsigned sb = ring + (hu % 5) * K2_SLOT;
      const int un = hu >> 1;
      const bool att = un >= 7;
      const int c = un;
      if (!(hu & 1)) {
        if (!att) {
#pragma unroll
          for (int u = 0; u < 8; ++u) dma16s(aoff[u & 1], ws + (size_t)u * 8 * F_ * 2 + 128 * c, sb + u * 1024);
        } else {
#pragma unroll
          for (int u = 0; u < 8; ++u) dma16s(qoff[u & 1], ws + (size_t)u * 8 * 1024 + 128 * (un - 7), sb + u * 1024);
        }
      } else {
        if (!att) {
          const char* wb = w2 + (size_t)(448 * w + 64 * c) * 896;
#pragma unroll
          for (int u = 0; u < 8; ++u) {
            const int kb = 32 * (u >> 2) + ((u & 1) ? 16 : 0) + ((u & 2) ? 4 : 0);
            dma16s(boff, wb + kb * 896, sb + u * 1024);
          }
        } else {
#pragma unroll
          for (int u = 0; u < 8; ++u) {
            const int e = min(8 * (u & 3) + lr, 27);
            const unsigned vo = (unsigned)(28 * (4 * w + 2 * (un - 7) + (u >> 2)) + e) * 896 + bcol;
            dma16s(vo, wo, sb + u * 1024);
          }
        }
      }
    };
    issue(0); issue(1); issue(2); issue(3); issue(4);
#pragma unroll
    for (int un = 0; un < 9; ++un) {
      if (un == 0) WAITVM(24); else if (un < 8) WAITVM(8); else WAITVM(0);
      BARRIER();
      if (un >= 1) {
        if (2 * un + 3 < 18) issue(2 * un + 3);
        if (2 * un + 4 < 18) issue(2 * un + 4);
      }
    }
  } else {
    if (b < 64 && w == 3) {
      const int head = b >> 2, pos = 8 * (b & 3) + (lane >> 3), d4 = (lane & 7) * 4;
      const float* P = (const float*)(ws + WS_P) + (size_t)head * 8 * 1024 + pos * 32 + d4;
      f32x4 v[8];
#pragma unroll
      for (int t = 0; t < 8; ++t) v[t] = *(const f32x4*)(P + t * 1024);
      f32x4 sacc = v[0];
#pragma unroll
      for (int t = 1; t < 8; ++t) sacc += v[t];
      const int e = pos >= 4 ? pos - 4 : pos + 28;
      unsigned long long o = (unsigned long long)pack_bf16(sacc[0], sacc[1]) |
                             ((unsigned long long)pack_bf16(sacc[2], sacc[3]) << 32);
      __hip_atomic_store((unsigned long long*)(ws + WS_KVT + ((size_t)(head * 32 + e) * 32 + d4) * 2), o,
                         __ATOMIC_RELAXED, __HIP_MEMORY_SCOPE_AGENT);
      WAITVM(0);
      if (lane == 0) __hip_atomic_fetch_add((unsigned*)(ws + WS_CNT), 1u, __ATOMIC_RELAXED, __HIP_MEMORY_SCOPE_AGENT);
    }
    const int sig = (2 * g + (q >> 1)) & 3;
    unsigned lb[4], la[2];
#pragma unroll
    for (int xx = 0; xx < 4; ++xx) lb[xx] = ring + (4 * g + q) * 128 + ((xx ^ sig) * 32) + 8 * p;
#pragma unroll
    for (int ks = 0; ks < 2; ++ks) la[ks] = ring + m * 128 + (((4 * ks + g) ^ ((m >> 1) & 7)) * 16);
#pragma unroll
    for (int i = 0; i < 4; ++i)
#pragma unroll
      for (int t = 0; t < 4; ++t) { acc[i][t] = f32x4{0.f, 0.f, 0.f, 0.f}; acs[i][t] = f32x4{0.f, 0.f, 0.f, 0.f}; }
    u32x4 kvf[4][3];
    unsigned cnt0;
#pragma unroll
    for (int un = 0; un < 9; ++un) {
      const int sa = ((2 * un) % 5) * K2_SLOT, sbb = ((2 * un + 1) % 5) * K2_SLOT;
      BARRIER();
      if (un == 5) {
        asm volatile("global_load_dword %0, %1, %2 sc1" : "=v"(cnt0) : "v"((unsigned)WS_CNT), "s"(ws) : "memory");
      }
      if (un == 6) {
        asm volatile("s_waitcnt vmcnt(0)" : "+v"(cnt0) :: "memory");
        if (__builtin_amdgcn_readfirstlane(cnt0) < 64u) {
          const unsigned* cnt = (const unsigned*)(ws + WS_CNT);
          while (__hip_atomic_load(cnt, __ATOMIC_RELAXED, __HIP_MEMORY_SCOPE_AGENT) < 64u) __builtin_amdgcn_s_sleep(8);
        }
        const unsigned ko = (unsigned)(WS_KVT + ((size_t)(4 * w * 32 + m) * 32 + 8 * g) * 2);
        const unsigned zo = (unsigned)(WS_KVT + ((size_t)(4 * w * 32 + 28) * 32 + 8 * g) * 2);
#pragma unroll
        for (int h4 = 0; h4 < 4; ++h4) {
#pragma unroll
          for (int ec = 0; ec < 2; ++ec)
            asm volatile("global_load_dwordx4 %0, %1, %2 offset:%3 sc1" : "=v"(kvf[h4][ec]) : "v"(ko + (h4 >> 1) * 4096),
                         "s"(ws), "i"(((h4 & 1) * 32 + ec * 16) * 64) : "memory");
          asm volatile("global_load_dwordx4 %0, %1, %2 offset:%3 sc1" : "=v"(kvf[h4][2]) : "v"(zo + (h4 >> 1) * 4096),
                       "s"(ws), "i"((h4 & 1) * 2048) : "memory");
        }
      }
      if (un == 7) {
        asm volatile("s_waitcnt vmcnt(0)" : "+v"(kvf[0][0]), "+v"(kvf[0][1]), "+v"(kvf[1][0]), "+v"(kvf[1][1]), "+v"(kvf[2][0]),
                     "+v"(kvf[2][1]), "+v"(kvf[3][0]), "+v"(kvf[3][1]), "+v"(kvf[0][2]), "+v"(kvf[1][2]), "+v"(kvf[2][2]),
                     "+v"(kvf[3][2]) :: "memory");
        __builtin_amdgcn_sched_barrier(0);
      }
      if (un < 7) {
#pragma unroll
        for (int ks = 0; ks < 2; ++ks) {
          bf16x8 wf[4], af[4];
#pragma unroll
          for (int t = 0; t < 4; ++t) wf[t] = cat8(tr_read(lb[t] + sbb + ks * 4096), tr_read(lb[t] + sbb + ks * 4096 + 2048));
#pragma unroll
          for (int i = 0; i < 4; ++i) af[i] = *(LAS bf16x8*)(uintptr_t)(la[ks] + sa + i * 2048);
#pragma unroll
          for (int t = 0; t < 4; ++t)
#pragma unroll
            for (int i = 0; i < 4; ++i) acc[i][t] = mfma16(wf[t], af[i], acc[i][t]);
        }
      } else {
        f32x4 n0[2][4], n1[2][4], dz[2][4];
#pragma unroll
        for (int ks = 0; ks < 2; ++ks) {
          const int h4 = 2 * (un - 7) + ks;
#pragma unroll
          for (int i = 0; i < 4; ++i) {
            const bf16x8 af = *(LAS bf16x8*)(uintptr_t)(la[ks] + sa + i * 2048);
            dz[ks][i] = mfma16(__builtin_bit_cast(bf16x8, kvf[h4][2]), af, f32x4{0.f, 0.f, 0.f, 0.f});
            n1[ks][i] = mfma16(__builtin_bit_cast(bf16x8, kvf[h4][1]), af, f32x4{0.f, 0.f, 0.f, 0.f});
            n0[ks][i] = mfma16(__builtin_bit_cast(bf16x8, kvf[h4][0]), af, f32x4{0.f, 0.f, 0.f, 0.f});
          }
        }
#pragma unroll
        for (int ks = 0; ks < 2; ++ks) {
          bf16x8 wf[4];
#pragma unroll
          for (int t = 0; t < 4; ++t) wf[t] = cat8(tr_read(lb[t] + sbb + ks * 4096), tr_read(lb[t] + sbb + ks * 4096 + 2048));
#pragma unroll
          for (int i = 0; i < 4; ++i) {
            const float rden = __builtin_amdgcn_rcpf(dz[ks][i][0] + 1e-6f);
            const float rd1 = g == 3 ? 0.f : rden;
            u32x4 ob = {pack_bf16(n0[ks][i][0] * rden, n0[ks][i][1] * rden), pack_bf16(n0[ks][i][2] * rden, n0[ks][i][3] * rden),
                        pack_bf16(n1[ks][i][0] * rd1, n1[ks][i][1] * rd1), pack_bf16(n1[ks][i][2] * rd1, n1[ks][i][3] * rd1)};
            const bf16x8 of = __builtin_bit_cast(bf16x8, ob);
#pragma unroll
            for (int t = 0; t < 4; ++t) acs[i][t] = mfma16(wf[t], of, acs[i][t]);
          }
        }
      }
      __builtin_amdgcn_sched_barrier(0);
    }
  }

  __syncthreads();
  if (w8 < 4) {
#pragma unroll
    for (int i = 0; i < 4; ++i)
#pragma unroll
      for (int t = 0; t < 4; ++t) {
        char* pb = smem + (16 * i + m) * K2_RSTR + (16 * t + 4 * g) * 4;
        *(f32x4*)(pb + (2 * w) * K2_PART) = acc[i][t];
        *(f32x4*)(pb + (2 * w + 1) * K2_PART) = acs[i][t];
      }
  }
  WAITVM(0);
  __syncthreads();
  asm volatile("" : "+v"(xv));
  {
    const int row = tid >> 3, n0 = (tid & 7) * 8;
    unsigned res[4];
#pragma unroll
    for (int v4 = 0; v4 < 2; ++v4) {
      const char* pb = smem + row * K2_RSTR + (n0 + 4 * v4) * 4;
      f32x4 so = *(const f32x4*)(pb), ss = *(const f32x4*)(pb + K2_PART);
#pragma unroll
      for (int u = 1; u < 4; ++u) {
        so += *(const f32x4*)(pb + (2 * u) * K2_PART);
        ss += *(const f32x4*)(pb + (2 * u + 1) * K2_PART);
      }
      float y[4];
#pragma unroll
      for (int r = 0; r < 4; ++r) {
        const unsigned xu = xv[2 * v4 + (r >> 1)];
        const float xf = (r & 1) ? bf_hi(xu) : bf_lo(xu);
        y[r] = xf + ss[r] * silu_f(so[r]);
      }
      res[2 * v4] = pack_bf16(y[0], y[1]);
      res[2 * v4 + 1] = pack_bf16(y[2], y[3]);
    }
    *(u32x4*)(out + go) = u32x4{res[0], res[1], res[2], res[3]};
  }
}

extern "C" void kernel_launch(void* const* d_in, const int* in_sizes, int n_in,
                              void* d_out, int out_size, void* d_ws, size_t ws_size,
                              hipStream_t stream) {
  const char* x    = (const char*)d_in[0];
  const char* wqkv = (const char*)d_in[1];
  const char* wo   = (const char*)d_in[2];
  const char* w1   = (const char*)d_in[4];
  const char* w3   = (const char*)d_in[5];
  const char* w2   = (const char*)d_in[6];
  k1_kernel<<<256, 1024, 0, stream>>>(x, wqkv, w1, w3, (char*)d_ws, w2, wo);
  k2_kernel<<<224, 512, 0, stream>>>(x, wo, w2, (char*)d_ws, (char*)d_out);
}
```
